```python
import torch

def _softmax(x, axis=-1):
    return torch.softmax(x, dim=axis)
def _gelu(x):
    return torch.nn.functional.gelu(x, approximate="tanh")
def _silu(x):
    return torch.nn.functional.silu(x)
def _sigmoid(x):
    return torch.sigmoid(x)
def _layernorm(x, axis=-1, eps=1e-5):
    m = x.mean(dim=axis, keepdim=True)
    v = x.var(dim=axis, keepdim=True, unbiased=False)
    return (x - m) / torch.sqrt(v + eps)
def _scatter_add(tgt, idx, src, axis=0):
    return tgt.clone().index_add_(axis, idx, src)
def _segment_sum(src, seg, n):
    out = torch.zeros((n,) + src.shape[1:], dtype=src.dtype)
    return out.index_add_(0, seg, src)
def _pad(x, pw):
    flat = tuple(v for pair in reversed(pw) for v in pair)
    return torch.nn.functional.pad(x, flat)

kernel_name = 'sequential_preLN__grouped_kv_mqa__moe_arch_mod__prompt'

def setup_inputs():

    _w = (lambda a: (lambda t: t.to(torch.bfloat16) if t.is_floating_point() else t)(torch.as_tensor(a)))
    def _A_setup():
        B, S, D, H = 1, 2048, 448, 16
        Dh = D // H
        return {
            "x": _w((lambda *s: torch.randn(*s, dtype=torch.bfloat16) * 0.05)(B, S, D)),
            "wq": _w((lambda *s: torch.randn(*s, dtype=torch.bfloat16) * 0.05)(D, H * Dh)),
            "wk": _w((lambda *s: torch.randn(*s, dtype=torch.bfloat16) * 0.05)(D, Dh)),
            "wv": _w((lambda *s: torch.randn(*s, dtype=torch.bfloat16) * 0.05)(D, Dh)),
            "wo": _w((lambda *s: torch.randn(*s, dtype=torch.bfloat16) * 0.05)(H * Dh, D)),
        }
    _A_in = _A_setup()

    def _B_setup():
        B, S, D, F, C = 1, 2048, 448, 1792, 2
        return {
            "x": _w((lambda *s: torch.randn(*s, dtype=torch.bfloat16) * 0.05)(B, S, D)),
            "w_r": _w((lambda *s: torch.randn(*s, dtype=torch.bfloat16) * 0.05)(D)),
            "w_gate": _w((lambda *s: torch.randn(*s, dtype=torch.bfloat16) * 0.05)(D, F)),
            "w_up": _w((lambda *s: torch.randn(*s, dtype=torch.bfloat16) * 0.05)(D, F)),
            "w_down": _w((lambda *s: torch.randn(*s, dtype=torch.bfloat16) * 0.05)(F, D)),
        }
    _B_in = _B_setup()

    return {
        "A_x": _A_in["x"],
        "A_wq": _A_in["wq"],
        "A_wk": _A_in["wk"],
        "A_wv": _A_in["wv"],
        "A_wo": _A_in["wo"],
        "B_x": _B_in["x"],
        "B_w_r": _B_in["w_r"],
        "B_w_gate": _B_in["w_gate"],
        "B_w_up": _B_in["w_up"],
        "B_w_down": _B_in["w_down"],
    }

def reference(A_x, A_wq, A_wk, A_wv, A_wo, B_x, B_w_r, B_w_gate, B_w_up, B_w_down):
    def _A_ref(x, wq, wk, wv, wo):
        B, S, D = x.shape
        H = 16
        Dh = D // H
        q = (lambda x, *d: x.permute(*d))((x @ wq).reshape(B, S, H, Dh), 0, 2, 1, 3)
        k = x @ wk
        v = x @ wv
        scores = torch.einsum("bhqd,bkd->bhqk", q, k) * (Dh ** -0.5)
        p = _softmax(scores, axis=-1)
        o = torch.einsum("bhqk,bkd->bhqd", p, v)
        return (lambda x, *d: x.permute(*d))(o, 0, 2, 1, 3).reshape(B, S, H * Dh) @ wo

    def _B_ref(x, w_r, w_gate, w_up, w_down):
        B, S, D = x.shape
        C = 2
        r = x @ w_r
        idx = torch.argsort(-r, axis=-1)[:, :C]
        idx_e = (lambda x, n, axis: torch.repeat_interleave(x, n, dim=axis))(idx.reshape(B, C, 1), D, axis=2)
        x_sel = (lambda x, i, axis: torch.take_along_dim(x, i, dim=axis))(x, idx_e, axis=1)
        g = _silu(x_sel @ w_gate)
        u = x_sel @ w_up
        y = (g * u) @ w_down
        r_sel = _sigmoid((lambda x, i, axis: torch.take_along_dim(x, i, dim=axis))(r, idx, axis=1))
        y = y * r_sel.reshape(B, C, 1)
        b_off = (torch.arange(B) * S).reshape(B, 1)
        flat_idx = (idx + b_off).reshape(B * C)
        zero = (x * 0.0).reshape(B * S, D)
        upd = _scatter_add(zero, flat_idx, y.reshape(B * C, D), axis=0)
        return x + upd.reshape(B, S, D)

    x = A_x
    h = x + _A_ref(_layernorm(x, axis=-1), A_wq, A_wk, A_wv, A_wo)
    h2 = h + _B_ref(_layernorm(h, axis=-1), B_w_r, B_w_gate, B_w_up, B_w_down)
    return h2
```

```cpp
#include <hip/hip_runtime.h>
#include <stdint.h>

typedef unsigned short u16;
typedef __bf16 bf16x8_t __attribute__((ext_vector_type(8)));
typedef __bf16 bf16x16_t __attribute__((ext_vector_type(16)));
typedef __bf16 bf16x2_t __attribute__((ext_vector_type(2)));
typedef float f32x16 __attribute__((ext_vector_type(16)));
typedef float f32x8 __attribute__((ext_vector_type(8)));
typedef float f32x4 __attribute__((ext_vector_type(4)));
typedef float f32x2 __attribute__((ext_vector_type(2)));
typedef unsigned u32x4 __attribute__((ext_vector_type(4)));
typedef unsigned u32x2 __attribute__((ext_vector_type(2)));
typedef short s16x4 __attribute__((ext_vector_type(4)));
#define LAS __attribute__((address_space(3)))
#define GAS __attribute__((address_space(1)))

constexpr int S = 2048, D = 448, NH = 16, DH = 28, FF = 1792;
constexpr float QSCALE = 0.18898223650461362f * 1.4426950408889634f;
constexpr float EPS = 1e-5f;

constexpr size_t WS_QF = 0;
constexpr size_t WS_KV = WS_QF + 256 * 8192;
constexpr size_t WS_WO = WS_KV + 64 * 4096;
constexpr size_t WS_CAND = WS_WO + 392 * 1024 + 16384;
constexpr size_t WS_ROWS = WS_CAND + 4096;
constexpr size_t WS_ACT = WS_ROWS + 256 * 2 * 2 * 896;

#define MFMA32(a, b, c) __builtin_amdgcn_mfma_f32_32x32x16_bf16(__builtin_bit_cast(bf16x8_t, a), __builtin_bit_cast(bf16x8_t, b), c, 0, 0, 0)
#define MFMA16(a, b, c) __builtin_amdgcn_mfma_f32_16x16x32_bf16(__builtin_bit_cast(bf16x8_t, a), __builtin_bit_cast(bf16x8_t, b), c, 0, 0, 0)
#define SMF(a, b8, c) __builtin_amdgcn_smfmac_f32_32x32x32_bf16(__builtin_bit_cast(bf16x8_t, a), __builtin_bit_cast(bf16x16_t, b8), c, 0xDDDD, 0, 0)
#define LO8(x) __builtin_shufflevector(x, x, 0, 1, 2, 3, 4, 5, 6, 7)
#define HI8(x) __builtin_shufflevector(x, x, 8, 9, 10, 11, 12, 13, 14, 15)
#define SBAR() __builtin_amdgcn_sched_barrier(0)
#define PIN(x) asm volatile("" : "+v"(x))

__device__ __forceinline__ unsigned pk2(float lo, float hi) {
    bf16x2_t v = {(__bf16)lo, (__bf16)hi};
    return __builtin_bit_cast(unsigned, v);
}
__device__ __forceinline__ float bfr(float x) { return __uint_as_float(pk2(x, 0.f) << 16); }
__device__ __forceinline__ float blo(unsigned u) { return __uint_as_float(u << 16); }
__device__ __forceinline__ float bhi(unsigned u) { return __uint_as_float(u & 0xffff0000u); }
__device__ __forceinline__ u32x2 trread(const char LAS* p) {
    return __builtin_bit_cast(u32x2, __builtin_amdgcn_ds_read_tr16_b64_v4i16((LAS s16x4*)p));
}
__device__ __forceinline__ float wsum(float v) {
#pragma unroll
    for (int o = 1; o < 64; o <<= 1) v += __shfl_xor(v, o);
    return v;
}

constexpr int K1_XN = 0;
constexpr int K1_WQ = 64 * 912;
constexpr int K1_WKV = K1_WQ + 50176 + 64;
constexpr int K1_LDS = K1_WKV + 25600 + 64;

__global__ __launch_bounds__(512, 1) void k1_qkv(const u16* __restrict__ x, const u16* __restrict__ wq, const u16* __restrict__ wk,
                                                 const u16* __restrict__ wv, const u16* __restrict__ wo, char* __restrict__ ws) {
    extern __shared__ __attribute__((aligned(16))) char smem[];
    const int tid = threadIdx.x, lane = tid & 63, w = __builtin_amdgcn_readfirstlane(tid >> 6);
    const int tg = blockIdx.x & 31, hp = blockIdx.x >> 5;
    u32x4 xv[7];
    {
        const u16* xr = x + (size_t)(64 * tg + 8 * w + (lane >> 3)) * D + (lane & 7) * 8;
#pragma unroll
        for (int i = 0; i < 7; ++i) xv[i] = *(const u32x4*)(xr + 64 * i);
    }
    const int fi = ((hp - 4) * 32 + tg) * 4 + (w - 4);
    const bool wojob = hp >= 4 && w >= 4 && fi < 392;
    u16 wov[8];
    if (wojob) {
        const int nt = fi / 14, kk = fi - nt * 14;
        const u16* p = wo + (size_t)(32 * kk + 8 * (lane >> 4)) * D + 16 * nt + (lane & 15);
#pragma unroll
        for (int j = 0; j < 8; ++j) wov[j] = p[j * D];
    }
    for (int i = w; i < 49; i += 8) {
        const int c = i * 64 + lane, k = c / 7, ch = c - k * 7;
        __builtin_amdgcn_global_load_lds((const unsigned*)(wq + (size_t)k * D + hp * 56 + ch * 8), (unsigned*)(smem + K1_WQ + i * 1024), 16, 0, 0);
    }
    if (hp < 4) {
        const u16* wsrc = (hp >> 1) ? wv : wk;
        for (int i = w; i < 25; i += 8) {
            const int c = min(i * 64 + lane, 1567);
            __builtin_amdgcn_global_load_lds((const unsigned*)(wsrc + c * 8), (unsigned*)(smem + K1_WKV + i * 1024), 16, 0, 0);
        }
    }
    {
        float f[56];
#pragma unroll
        for (int i = 0; i < 7; ++i)
#pragma unroll
            for (int e = 0; e < 4; ++e) { f[i * 8 + 2 * e] = blo(xv[i][e]); f[i * 8 + 2 * e + 1] = bhi(xv[i][e]); }
        float s = 0.f;
#pragma unroll
        for (int i = 0; i < 56; ++i) s += f[i];
        s += __shfl_xor(s, 1); s += __shfl_xor(s, 2); s += __shfl_xor(s, 4);
        const float mean = s * (1.f / D);
        float q = 0.f;
#pragma unroll
        for (int i = 0; i < 56; ++i) { const float d = f[i] - mean; q = fmaf(d, d, q); }
        q += __shfl_xor(q, 1); q += __shfl_xor(q, 2); q += __shfl_xor(q, 4);
        const float mb = bfr(mean), sd = bfr(sqrtf(bfr(bfr(q * (1.f / D)) + EPS)));
        const float rs = 1.f / sd;
        char* dst = smem + K1_XN + (8 * w + (lane >> 3)) * 912 + (lane & 7) * 16;
#pragma unroll
        for (int i = 0; i < 7; ++i) {
            u32x4 o;
#pragma unroll
            for (int e = 0; e < 4; ++e) o[e] = pk2((f[i * 8 + 2 * e] - mb) * rs, (f[i * 8 + 2 * e + 1] - mb) * rs);
            *(u32x4*)(dst + 128 * i) = o;
        }
    }
    if (wojob) {
        u32x4 o;
#pragma unroll
        for (int e = 0; e < 4; ++e) o[e] = (unsigned)wov[2 * e] | ((unsigned)wov[2 * e + 1] << 16);
        *(u32x4*)(ws + WS_WO + (size_t)fi * 1024 + lane * 16) = o;
    }
    __syncthreads();
    const char LAS* sm = (const char LAS*)smem;
    if (w < 4) {
        const int tt = w & 1, hl = w >> 1, hd = 2 * hp + hl, hi = lane >> 5;
        const char LAS* ap = sm + K1_WQ + (8 * hi + ((lane & 15) >> 2)) * 112 + hl * 56 + ((lane >> 4) & 1) * 32 + (lane & 3) * 8;
        const char LAS* bp = sm + K1_XN + (32 * tt + (lane & 31)) * 912 + hi * 16;
        f32x16 acc = {};
#pragma unroll 7
        for (int kk = 0; kk < 28; ++kk) {
            const u32x2 a0 = trread(ap + kk * 16 * 112), a1 = trread(ap + kk * 16 * 112 + 4 * 112);
            const u32x4 a = {a0[0], a0[1], a1[0], a1[1]};
            const u32x4 b = *(const LAS u32x4*)(bp + kk * 32);
            acc = MFMA32(a, b, acc);
        }
        const int T = 64 * tg + 32 * tt + (lane & 31);
        char* dst = ws + WS_QF + (size_t)(((T >> 3) * 4 + (hd >> 2)) * 2) * 1024 + ((hd & 3) * 8 + (T & 7) + 32 * hi) * 16;
        u32x4 o0, o1;
#pragma unroll
        for (int e = 0; e < 4; ++e) {
            o0[e] = pk2(acc[2 * e] * QSCALE, acc[2 * e + 1] * QSCALE);
            o1[e] = pk2(acc[8 + 2 * e] * QSCALE, acc[8 + 2 * e + 1] * QSCALE);
        }
        if (hi) { o1[2] = 0u; o1[3] = 0u; }
        *(u32x4*)dst = o0;
        *(u32x4*)(dst + 1024) = o1;
    } else if (hp < 4) {
        const int isv = hp >> 1, tt = hp & 1, i = (w - 4) & 1, jx = (w - 4) >> 1, g = lane >> 4, c = lane & 15;
        const char LAS* wp = sm + K1_WKV + (8 * g + (c >> 2)) * 56 + i * 32 + (lane & 3) * 8;
        const char LAS* xp = sm + K1_XN + (32 * tt + 16 * jx + c) * 912 + g * 16;
        f32x4 acc = {};
#pragma unroll 7
        for (int kk = 0; kk < 14; ++kk) {
            const u32x2 a0 = trread(wp + kk * 32 * 56), a1 = trread(wp + kk * 32 * 56 + 4 * 56);
            const u32x4 wf = {a0[0], a0[1], a1[0], a1[1]};
            const u32x4 xf = *(const LAS u32x4*)(xp + kk * 64);
            if (isv) acc = MFMA16(xf, wf, acc); else acc = MFMA16(wf, xf, acc);
        }
        u32x2 o = {pk2(acc[0], acc[1]), pk2(acc[2], acc[3])};
        char* kvt = ws + WS_KV + (size_t)(2 * tg + tt) * 4096;
        if (!isv) {
            if (i == 1 && g == 3) { o[0] = 0x00003F80u; o[1] = 0u; }
            *(u32x2*)(kvt + i * 1024 + (16 * jx + c + 32 * (g & 1)) * 16 + 8 * (g >> 1)) = o;
        } else {
            if (i == 1 && c >= 12) { o[0] = o[1] = (c == 12) ? 0x3F803F80u : 0u; }
            *(u32x2*)(kvt + (2 + jx) * 1024 + (16 * i + c + 32 * (g >> 1)) * 16 + 8 * (g & 1)) = o;
        }
    }
}

constexpr int K2_PART = 0;
constexpr int K2_M = 65536;
constexpr int K2_O = K2_M + 2048;
constexpr int K2_A = K2_O + 8 * 912;
constexpr int K2_R = K2_A + 8 * 896;
constexpr int K2_LDS = K2_R + 64;

#define EXP4(v, i) do { v[i] = __builtin_amdgcn_exp2f(v[i]); v[i + 1] = __builtin_amdgcn_exp2f(v[i + 1]); v[i + 2] = __builtin_amdgcn_exp2f(v[i + 2]); v[i + 3] = __builtin_amdgcn_exp2f(v[i + 3]); PIN(v); SBAR(); } while (0)

__device__ __forceinline__ float max16(const f32x16& v) {
    float a = fmaxf(fmaxf(v[0], v[1]), v[2]), b = fmaxf(fmaxf(v[3], v[4]), v[5]);
    a = fmaxf(fmaxf(a, v[6]), v[7]); b = fmaxf(fmaxf(b, v[8]), v[9]);
    a = fmaxf(fmaxf(a, v[10]), v[11]); b = fmaxf(fmaxf(b, v[12]), v[13]);
    a = fmaxf(fmaxf(a, v[14]), v[15]);
    a = fmaxf(a, b);
    return fmaxf(a, __shfl_xor(a, 32));
}

__global__ __launch_bounds__(256, 1) void k2_attn(const u16* __restrict__ x, const u16* __restrict__ wr, char* __restrict__ ws, u16* __restrict__ out) {
    extern __shared__ __attribute__((aligned(16))) char smem[];
    const int tid = threadIdx.x, lane = tid & 63, w = __builtin_amdgcn_readfirstlane(tid >> 6), hi = lane >> 5;
    const int wg = blockIdx.x, rot = (wg >> 3) & 15;
    const char* kvb = ws + WS_KV + (size_t)w * 16 * 4096 + lane * 16;
    const char* wob = ws + WS_WO + (size_t)w * 98 * 1024 + lane * 16;
    u32x4 q[4][2], kf[4][2], vf[4][2];
    {
        const char* qp = ws + WS_QF + (size_t)wg * 8192 + lane * 16;
        const char* t0 = kvb + ((rot)&15) * 4096;
        kf[0][0] = *(const u32x4*)(t0); kf[0][1] = *(const u32x4*)(t0 + 1024);
#pragma unroll
        for (int j = 0; j < 4; ++j) { q[j][0] = *(const u32x4*)(qp + j * 2048); q[j][1] = *(const u32x4*)(qp + j * 2048 + 1024); }
        vf[0][0] = *(const u32x4*)(t0 + 2048); vf[0][1] = *(const u32x4*)(t0 + 3072);
#pragma unroll
        for (int a = 1; a < 4; ++a) {
            const char* t = kvb + ((a + rot) & 15) * 4096;
            kf[a][0] = *(const u32x4*)(t); kf[a][1] = *(const u32x4*)(t + 1024);
            vf[a][0] = *(const u32x4*)(t + 2048); vf[a][1] = *(const u32x4*)(t + 3072);
        }
    }
    const int l56 = min(lane, 55);
    const u32x4 xr0 = *(const u32x4*)(x + (size_t)(8 * wg + 2 * w) * D + l56 * 8);
    const u32x4 xr1 = *(const u32x4*)(x + (size_t)(8 * wg + 2 * w + 1) * D + l56 * 8);
    const u32x4 wrv = *(const u32x4*)(wr + l56 * 8);

    const f32x16 zero = {};
    f32x16 sb[4], o[4];
    float mrow[4];
#pragma unroll
    for (int j = 0; j < 4; ++j) { sb[j] = MFMA32(kf[0][0], q[j][0], zero); sb[j] = MFMA32(kf[0][1], q[j][1], sb[j]); o[j] = zero; }
#pragma unroll
    for (int j = 0; j < 4; ++j) {
        mrow[j] = bfr(max16(sb[j]));
        if (hi) q[j][1][2] = __float_as_uint(-mrow[j]) >> 16;
    }
#pragma unroll
    for (int i = 0; i < 16; ++i) sb[0][i] -= mrow[0];
    sb[3] = zero;

#define STAGE(J, KS, JN, VP, JP, VC, LOADS) do { \
        sb[JN] = MFMA32(kf[KS][0], q[JN][0], zero); SBAR(); \
        EXP4(sb[J], 0); \
        o[JP] = SMF(vf[VP][1], HI8(sb[JP]), o[JP]); SBAR(); \
        EXP4(sb[J], 4); \
        sb[JN] = MFMA32(kf[KS][1], q[JN][1], sb[JN]); SBAR(); \
        LOADS; SBAR(); \
        EXP4(sb[J], 8); \
        o[J] = SMF(vf[VC][0], LO8(sb[J]), o[J]); SBAR(); \
        EXP4(sb[J], 12); \
    } while (0)
#define TILE(A, AN, AP) do { \
        STAGE(0, A, 1, AP, 3, A, vf[AP][0] = *(const u32x4*)(pprev + 2048)); \
        STAGE(1, A, 2, A, 0, A, vf[AP][1] = *(const u32x4*)(pprev + 3072)); \
        STAGE(2, A, 3, A, 1, A, (void)0); \
        { const int ni = it * 4 + A + 4; pprev = ni < 16 ? kvb + ((ni + rot) & 15) * 4096 : wob + (ni - 16) * 4096; } \
        STAGE(3, AN, 0, A, 2, A, (kf[A][0] = *(const u32x4*)(pprev), kf[A][1] = *(const u32x4*)(pprev + 1024))); \
    } while (0)
    const char* pprev = kvb + ((3 + rot) & 15) * 4096;
    for (int it = 0; it < 4; ++it) {
        TILE(0, 1, 3);
        TILE(1, 2, 0);
        TILE(2, 3, 1);
        TILE(3, 0, 2);
    }
    o[3] = SMF(vf[3][1], HI8(sb[3]), o[3]);

    {
        bool bad = false;
#pragma unroll
        for (int j = 0; j < 4; ++j) {
            float a = 0.f;
#pragma unroll
            for (int i = 0; i < 16; ++i) a = fmaxf(a, fabsf(o[j][i]));
            float nanchk = 0.f;
#pragma unroll
            for (int i = 0; i < 16; ++i) nanchk += o[j][i] * 0.f;
            bad |= !(a < 1.0e30f) || !(nanchk == 0.f) || (hi && !(o[j][12] < 1.0e18f));
        }
        if (__any(bad)) {
#pragma unroll
            for (int j = 0; j < 4; ++j) { if (hi) q[j][1][2] = 0u; o[j] = zero; mrow[j] = -INFINITY; }
            for (int t = 0; t < 16; ++t) {
                const char* tp = kvb + t * 4096;
                const u32x4 k0 = *(const u32x4*)(tp), k1 = *(const u32x4*)(tp + 1024), v0 = *(const u32x4*)(tp + 2048), v1 = *(const u32x4*)(tp + 3072);
#pragma unroll
                for (int j = 0; j < 4; ++j) {
                    f32x16 s = MFMA32(k0, q[j][0], zero);
                    s = MFMA32(k1, q[j][1], s);
                    const float mn = fmaxf(mrow[j], max16(s));
                    const float al = __builtin_amdgcn_exp2f(mrow[j] - mn);
                    mrow[j] = mn;
#pragma unroll
                    for (int i = 0; i < 16; ++i) { s[i] = __builtin_amdgcn_exp2f(s[i] - mn); o[j][i] *= al; }
                    o[j] = SMF(v0, LO8(s), o[j]);
                    o[j] = SMF(v1, HI8(s), o[j]);
                }
            }
        }
    }
#pragma unroll
    for (int j = 0; j < 4; ++j) {
        if (j != w) {
#pragma unroll
            for (int g = 0; g < 4; ++g)
                *(f32x4*)(smem + K2_PART + ((w * 4 + j) * 4 + g) * 1024 + lane * 16) = (f32x4){o[j][4 * g], o[j][4 * g + 1], o[j][4 * g + 2], o[j][4 * g + 3]};
        }
        if (!hi) *(float*)(smem + K2_M + ((w * 4 + j) * 32 + lane) * 4) = mrow[j];
    }
    f32x16 own = w == 0 ? o[0] : w == 1 ? o[1] : w == 2 ? o[2] : o[3];
    __syncthreads();
    {
        float mw[4];
#pragma unroll
        for (int v = 0; v < 4; ++v) mw[v] = *(const float*)(smem + K2_M + ((v * 4 + w) * 32 + (lane & 31)) * 4);
        const float M = fmaxf(fmaxf(mw[0], mw[1]), fmaxf(mw[2], mw[3]));
        f32x16 acc = zero;
#pragma unroll
        for (int v = 0; v < 4; ++v) {
            const float f = __builtin_amdgcn_exp2f(mw[v] - M);
            if (v == w) {
#pragma unroll
                for (int i = 0; i < 16; ++i) acc[i] = fmaf(own[i], f, acc[i]);
            } else {
#pragma unroll
                for (int g = 0; g < 4; ++g) {
                    const f32x4 p = *(const f32x4*)(smem + K2_PART + ((v * 4 + w) * 4 + g) * 1024 + lane * 16);
#pragma unroll
                    for (int e = 0; e < 4; ++e) acc[4 * g + e] = fmaf(p[e], f, acc[4 * g + e]);
                }
            }
        }
        const float l = __shfl(acc[12], (lane & 31) + 32);
        const float rl = 1.f / l;
        const int head = 4 * w + ((lane & 31) >> 3), t = lane & 7;
        char* dst = smem + K2_O + t * 912 + (head * 28 + 4 * hi) * 2;
#pragma unroll
        for (int g = 0; g < 4; ++g) {
            if (g < 3 || !hi) *(u32x2*)(dst + 16 * g) = (u32x2){pk2(acc[4 * g] * rl, acc[4 * g + 1] * rl), pk2(acc[4 * g + 2] * rl, acc[4 * g + 3] * rl)};
        }
    }
    __syncthreads();
    {
        const int g = lane >> 4, c = lane & 15;
        u32x4 bfr_[14];
#pragma unroll
        for (int kk = 0; kk < 14; ++kk) bfr_[kk] = *(const u32x4*)(smem + K2_O + (c & 7) * 912 + kk * 64 + g * 16);
#pragma unroll 1
        for (int nt = 0; nt < 7; ++nt) {
            u32x4 af[14];
#pragma unroll
            for (int kk = 0; kk < 14; ++kk) af[kk] = *(const u32x4*)(wob + (nt * 14 + kk) * 1024);
            f32x4 acc = {};
#pragma unroll
            for (int kk = 0; kk < 14; ++kk) acc = MFMA16(af[kk], bfr_[kk], acc);
            if (c < 8) *(u32x2*)(smem + K2_A + c * 896 + (112 * w + 16 * nt + 4 * g) * 2) = (u32x2){pk2(acc[0], acc[1]), pk2(acc[2], acc[3])};
        }
    }
    __syncthreads();
    u32x4 hrow[2], nrow[2];
    {
        const bool act = lane < 56;
#pragma unroll
        for (int tk = 0; tk < 2; ++tk) {
            const u32x4 av = *(const u32x4*)(smem + K2_A + (2 * w + tk) * 896 + l56 * 16);
            const u32x4 xv = tk ? xr1 : xr0;
            float h[8];
#pragma unroll
            for (int e = 0; e < 4; ++e) {
                hrow[tk][e] = pk2(blo(xv[e]) + blo(av[e]), bhi(xv[e]) + bhi(av[e]));
                h[2 * e] = blo(hrow[tk][e]); h[2 * e + 1] = bhi(hrow[tk][e]);
            }
            float s = 0.f;
#pragma unroll
            for (int e = 0; e < 8; ++e) s += h[e];
            const float mean = wsum(act ? s : 0.f) * (1.f / D);
            float qq = 0.f;
#pragma unroll
            for (int e = 0; e < 8; ++e) { const float d = h[e] - mean; qq = fmaf(d, d, qq); }
            const float var = wsum(act ? qq : 0.f) * (1.f / D);
            const float mb = bfr(mean), sd = bfr(sqrtf(bfr(bfr(var) + EPS)));
            const float rs = 1.f / sd;
            float r = 0.f;
            u32x4 ov;
#pragma unroll
            for (int e = 0; e < 4; ++e) {
                const unsigned dd = pk2(h[2 * e] - mb, h[2 * e + 1] - mb);
                nrow[tk][e] = pk2(blo(dd) * rs, bhi(dd) * rs);
                const float n0 = blo(nrow[tk][e]), n1 = bhi(nrow[tk][e]);
                r = fmaf(n0, blo(wrv[e]), r); r = fmaf(n1, bhi(wrv[e]), r);
                ov[e] = pk2(h[2 * e] + n0, h[2 * e + 1] + n1);
            }
            r = wsum(act ? r : 0.f);
            if (act) *(u32x4*)(out + (size_t)(8 * wg + 2 * w + tk) * D + lane * 8) = ov;
            if (lane == 0) *(float*)(smem + K2_R + (2 * w + tk) * 4) = r;
        }
    }
    __syncthreads();
    {
        float rv[8];
#pragma unroll
        for (int t = 0; t < 8; ++t) rv[t] = *(const float*)(smem + K2_R + t * 4);
#pragma unroll
        for (int tk = 0; tk < 2; ++tk) {
            const int me = 2 * w + tk;
            const float mine = *(const float*)(smem + K2_R + me * 4);
            int rank = 0;
#pragma unroll
            for (int t = 0; t < 8; ++t) rank += (rv[t] > mine || (rv[t] == mine && t < me)) ? 1 : 0;
            if (!(mine == mine)) rank = me;
            if (rank < 2) {
                char* rp = ws + WS_ROWS + (size_t)((wg * 2 + rank) * 2) * 896;
                if (lane < 56) { *(u32x4*)(rp + lane * 16) = hrow[tk]; *(u32x4*)(rp + 896 + lane * 16) = nrow[tk]; }
                if (lane == 0) *(u32x2*)(ws + WS_CAND + (wg * 2 + rank) * 8) = (u32x2){__float_as_uint(mine), (unsigned)(8 * wg + me)};
            }
        }
    }
}

__device__ __forceinline__ void top2(const char* ws, int lane, int& s0, int& s1, float& r0, float& r1, int& t0, int& t1) {
    unsigned long long b0 = 0ull, b1 = 0ull;
#pragma unroll
    for (int i = 0; i < 4; ++i) {
        const u32x4 v = *(const u32x4*)(ws + WS_CAND + (i * 64 + lane) * 16);
#pragma unroll
        for (int e = 0; e < 2; ++e) {
            const unsigned u = v[2 * e], mono = (u & 0x80000000u) ? ~u : (u | 0x80000000u);
            const unsigned long long key = ((unsigned long long)mono << 32) | (unsigned)(1023 - ((i * 64 + lane) * 2 + e));
            if (key > b0) { b1 = b0; b0 = key; } else if (key > b1) b1 = key;
        }
    }
#pragma unroll
    for (int o = 1; o < 64; o <<= 1) {
        const unsigned long long c0 = __shfl_xor(b0, o), c1 = __shfl_xor(b1, o);
        const unsigned long long hi0 = b0 > c0 ? b0 : c0, lo0 = b0 > c0 ? c0 : b0, m1 = b1 > c1 ? b1 : c1;
        b0 = hi0; b1 = lo0 > m1 ? lo0 : m1;
    }
    s0 = 1023 - (int)(b0 & 1023u); s1 = 1023 - (int)(b1 & 1023u);
    const u32x2 c0 = *(const u32x2*)(ws + WS_CAND + s0 * 8), c1 = *(const u32x2*)(ws + WS_CAND + s1 * 8);
    r0 = __uint_as_float(c0[0]); r1 = __uint_as_float(c1[0]); t0 = (int)c0[1]; t1 = (int)c1[1];
}

__global__ __launch_bounds__(256) void k3_gateup(const u16* __restrict__ wg_, const u16* __restrict__ wu_, char* __restrict__ ws) {
    __shared__ float red[4][64];
    const int tid = threadIdx.x, lane = tid & 63, w = tid >> 6;
    const int b = blockIdx.x, cb = (b & 7) * 14 + (b >> 3);
    const int ch = tid & 1, rg = tid >> 1;
    u32x4 gv[4], uv[4];
#pragma unroll
    for (int i = 0; i < 4; ++i) {
        const int row = min(rg + 128 * i, D - 1);
        gv[i] = *(const u32x4*)(wg_ + (size_t)row * FF + cb * 16 + ch * 8);
        uv[i] = *(const u32x4*)(wu_ + (size_t)row * FF + cb * 16 + ch * 8);
    }
    int s0, s1, t0, t1; float r0, r1;
    top2(ws, lane, s0, s1, r0, r1, t0, t1);
    const u16* n0 = (const u16*)(ws + WS_ROWS + (size_t)(s0 * 2 + 1) * 896);
    const u16* n1 = (const u16*)(ws + WS_ROWS + (size_t)(s1 * 2 + 1) * 896);
    float acc[32];
#pragma unroll
    for (int i = 0; i < 32; ++i) acc[i] = 0.f;
#pragma unroll
    for (int i = 0; i < 4; ++i) {
        const int row = rg + 128 * i;
        const float a0 = row < D ? blo(n0[min(row, D - 1)]) : 0.f, a1 = row < D ? blo(n1[min(row, D - 1)]) : 0.f;
#pragma unroll
        for (int e = 0; e < 4; ++e) {
            const float g0 = blo(gv[i][e]), g1 = bhi(gv[i][e]), u0 = blo(uv[i][e]), u1 = bhi(uv[i][e]);
            acc[2 * e] = fmaf(a0, g0, acc[2 * e]); acc[2 * e + 1] = fmaf(a0, g1, acc[2 * e + 1]);
            acc[8 + 2 * e] = fmaf(a0, u0, acc[8 + 2 * e]); acc[8 + 2 * e + 1] = fmaf(a0, u1, acc[8 + 2 * e + 1]);
            acc[16 + 2 * e] = fmaf(a1, g0, acc[16 + 2 * e]); acc[16 + 2 * e + 1] = fmaf(a1, g1, acc[16 + 2 * e + 1]);
            acc[24 + 2 * e] = fmaf(a1, u0, acc[24 + 2 * e]); acc[24 + 2 * e + 1] = fmaf(a1, u1, acc[24 + 2 * e + 1]);
        }
    }
#pragma unroll
    for (int st = 0; st < 5; ++st) {
        const int half = 16 >> st, bit = 32 >> st;
        const bool up = lane & bit;
#pragma unroll
        for (int i = 0; i < half; ++i) {
            const float keep = up ? acc[half + i] : acc[i], send = up ? acc[i] : acc[half + i];
            acc[i] = keep + __shfl_xor(send, bit);
        }
    }
    red[w][lane] = acc[0];
    __syncthreads();
    if (tid < 32) {
        const int tok = tid >> 4, c2 = (tid >> 3) & 1, e = tid & 7;
        const int lg = 2 * (tok * 16 + e) + c2, lu = 2 * (tok * 16 + 8 + e) + c2;
        const float g = bfr(red[0][lg] + red[1][lg] + red[2][lg] + red[3][lg]);
        const float u = bfr(red[0][lu] + red[1][lu] + red[2][lu] + red[3][lu]);
        const float sg = bfr(g / (1.f + __expf(-g)));
        ((float*)(ws + WS_ACT))[tok * FF + cb * 16 + c2 * 8 + e] = bfr(sg * u);
    }
}

__global__ __launch_bounds__(256) void k4_down(const u16* __restrict__ wd, const char* __restrict__ ws, u16* __restrict__ out) {
    __shared__ float red[4][16];
    const int tid = threadIdx.x, lane = tid & 63, w = tid >> 6;
    const int b = blockIdx.x, cb = (b & 7) * 7 + (b >> 3);
    u32x4 dv[7];
#pragma unroll
    for (int i = 0; i < 7; ++i) dv[i] = *(const u32x4*)(wd + (size_t)(tid + 256 * i) * D + cb * 8);
    const float* act = (const float*)(ws + WS_ACT);
    float acc[16];
#pragma unroll
    for (int i = 0; i < 16; ++i) acc[i] = 0.f;
#pragma unroll
    for (int i = 0; i < 7; ++i) {
        const float a0 = act[tid + 256 * i], a1 = act[FF + tid + 256 * i];
#pragma unroll
        for (int e = 0; e < 4; ++e) {
            const float d0 = blo(dv[i][e]), d1 = bhi(dv[i][e]);
            acc[2 * e] = fmaf(a0, d0, acc[2 * e]); acc[2 * e + 1] = fmaf(a0, d1, acc[2 * e + 1]);
            acc[8 + 2 * e] = fmaf(a1, d0, acc[8 + 2 * e]); acc[8 + 2 * e + 1] = fmaf(a1, d1, acc[8 + 2 * e + 1]);
        }
    }
    int s0, s1, t0, t1; float r0, r1;
    top2(ws, lane, s0, s1, r0, r1, t0, t1);
#pragma unroll
    for (int st = 0; st < 4; ++st) {
        const int half = 8 >> st, bit = 32 >> st;
        const bool up = lane & bit;
#pragma unroll
        for (int i = 0; i < half; ++i) {
            const float keep = up ? acc[half + i] : acc[i], send = up ? acc[i] : acc[half + i];
            acc[i] = keep + __shfl_xor(send, bit);
        }
    }
    acc[0] += __shfl_xor(acc[0], 1); acc[0] += __shfl_xor(acc[0], 2);
    if ((lane & 3) == 0) red[w][lane >> 2] = acc[0];
    __syncthreads();
    if (tid < 16) {
        const int tok = tid >> 3, e = tid & 7, col = cb * 8 + e;
        const float y = bfr(red[0][tid] + red[1][tid] + red[2][tid] + red[3][tid]);
        const float r = bfr(tok ? r1 : r0);
        const float sg = bfr(1.f / (1.f + __expf(-r)));
        const u16* rows = (const u16*)(ws + WS_ROWS + (size_t)((tok ? s1 : s0) * 2) * 896);
        const float h = blo(rows[col]), hn = blo(rows[448 + col]);
        const float o = h + bfr(hn + bfr(y * sg));
        out[(size_t)(tok ? t1 : t0) * D + col] = (u16)(pk2(o, 0.f) & 0xffffu);
    }
}

extern "C" void kernel_launch(void* const* d_in, const int* in_sizes, int n_in, void* d_out, int out_size, void* d_ws, size_t ws_size,
                              hipStream_t stream) {
    const u16* x = (const u16*)d_in[0];
    const u16* wq = (const u16*)d_in[1];
    const u16* wk = (const u16*)d_in[2];
    const u16* wv = (const u16*)d_in[3];
    const u16* wo = (const u16*)d_in[4];
    const u16* wr = (const u16*)d_in[6];
    const u16* wgate = (const u16*)d_in[7];
    const u16* wup = (const u16*)d_in[8];
    const u16* wdown = (const u16*)d_in[9];
    char* ws = (char*)d_ws;
    u16* out = (u16*)d_out;
    (void)hipFuncSetAttribute((const void*)k1_qkv, hipFuncAttributeMaxDynamicSharedMemorySize, K1_LDS);
    (void)hipFuncSetAttribute((const void*)k2_attn, hipFuncAttributeMaxDynamicSharedMemorySize, K2_LDS);
    k1_qkv<<<256, 512, K1_LDS, stream>>>(x, wq, wk, wv, wo, ws);
    k2_attn<<<256, 256, K2_LDS, stream>>>(x, wr, ws, out);
    k3_gateup<<<112, 256, 0, stream>>>(wgate, wup, ws);
    k4_down<<<56, 256, 0, stream>>>(wdown, ws, out);
}
```

```cpp
#include <hip/hip_runtime.h>
#include <stdint.h>

typedef unsigned short u16;
typedef __bf16 bf16x8_t __attribute__((ext_vector_type(8)));
typedef __bf16 bf16x16_t __attribute__((ext_vector_type(16)));
typedef __bf16 bf16x2_t __attribute__((ext_vector_type(2)));
typedef float f32x16 __attribute__((ext_vector_type(16)));
typedef float f32x4 __attribute__((ext_vector_type(4)));
typedef float f32x2 __attribute__((ext_vector_type(2)));
typedef unsigned u32x4 __attribute__((ext_vector_type(4)));
typedef unsigned u32x2 __attribute__((ext_vector_type(2)));
typedef short s16x4 __attribute__((ext_vector_type(4)));
#define LAS __attribute__((address_space(3)))

constexpr int S = 2048, D = 448, FF = 1792;
constexpr float QSCALE = 0.18898223650461362f * 1.4426950408889634f;
constexpr float EPS = 1e-5f;

constexpr unsigned WS_QF = 0;
constexpr unsigned WS_KV = WS_QF + 256 * 8192;
constexpr unsigned WS_WO = WS_KV + 64 * 4096;
constexpr unsigned WS_ACT = WS_WO + 368 * 1024;
constexpr unsigned WS_CAND = WS_ACT + 224 * 128;
constexpr unsigned WS_FLAG = WS_CAND + 256 * 128;
constexpr unsigned WS_EPOCH = WS_FLAG + 256 * 128;
constexpr unsigned WS_HROW = WS_EPOCH + 1024;
constexpr unsigned WS_END = WS_HROW + S * 1792;
constexpr int TEXT_BYTES = 25728;

#define PROF_DECL
#define STAMP(k)
#define PROF_FLUSH(kid)
#define PROF_STAMP_ASM(k)
#define PROF_ASM_OUT1
#define PROF_ASM_OUT2
#define PROF_ASM_OUT3

#define MFMA32(a, b, c) __builtin_amdgcn_mfma_f32_32x32x16_bf16(__builtin_bit_cast(bf16x8_t, a), __builtin_bit_cast(bf16x8_t, b), c, 0, 0, 0)
#define MFMA16(a, b, c) __builtin_amdgcn_mfma_f32_16x16x32_bf16(__builtin_bit_cast(bf16x8_t, a), __builtin_bit_cast(bf16x8_t, b), c, 0, 0, 0)
#define SMF(a, b8, c) __builtin_amdgcn_smfmac_f32_32x32x32_bf16(__builtin_bit_cast(bf16x8_t, a), __builtin_bit_cast(bf16x16_t, b8), c, 0xDDDD, 0, 0)
#define LO8(x) __builtin_shufflevector(x, x, 0, 1, 2, 3, 4, 5, 6, 7)
#define HI8(x) __builtin_shufflevector(x, x, 8, 9, 10, 11, 12, 13, 14, 15)
#define SBAR() __builtin_amdgcn_sched_barrier(0)
#define LBAR() asm volatile("s_waitcnt lgkmcnt(0)\n\ts_barrier" ::: "memory")
#define DRAIN() asm volatile("s_waitcnt vmcnt(0)" ::: "memory")
constexpr int SC1 = 16;
#define LDW(off) __builtin_amdgcn_raw_buffer_load_b128(rsw, off, 0, SC1)
#define X2(a) a a
#define X7(a) a a a a a a a
#define P1_DW "s_mov_b32 m0, s40\n s_add_u32 s40, s40, 1024\n global_load_lds_dwordx4 %[wa], off\n v_lshl_add_u64 %[wa], %[wa], 0, %[wst]\n"
#define P1_DX "s_mov_b32 m0, s41\n s_mov_b32 exec_hi, 0x00ffffff\n global_load_lds_dwordx4 %[xo], s[42:43]\n s_mov_b32 exec_hi, -1\n s_add_u32 s41, s41, 912\n s_add_u32 s42, s42, 896\n s_addc_u32 s43, s43, 0\n"

__device__ __forceinline__ unsigned pk2(float lo, float hi) {
    bf16x2_t v = {(__bf16)lo, (__bf16)hi};
    return __builtin_bit_cast(unsigned, v);
}
__device__ __forceinline__ float bfr(float x) { return __uint_as_float(pk2(x, 0.f) << 16); }
__device__ __forceinline__ float blo(unsigned u) { return __uint_as_float(u << 16); }
__device__ __forceinline__ float bhi(unsigned u) { return __uint_as_float(u & 0xffff0000u); }
__device__ __forceinline__ float dot2(unsigned a, unsigned b, float c) {
    return __builtin_amdgcn_fdot2_f32_bf16(__builtin_bit_cast(bf16x2_t, a), __builtin_bit_cast(bf16x2_t, b), c, false);
}
__device__ __forceinline__ u32x2 trread(const char LAS* p) {
    return __builtin_bit_cast(u32x2, __builtin_amdgcn_ds_read_tr16_b64_v4i16((LAS s16x4*)p));
}
__device__ __forceinline__ u32x4 trfrag(const char LAS* p, int rowstride4) {
    const u32x2 a0 = trread(p), a1 = trread(p + rowstride4);
    return (u32x4){a0[0], a0[1], a1[0], a1[1]};
}
template <int CTRL> __device__ __forceinline__ float dppf(float v) { return __uint_as_float(__builtin_amdgcn_update_dpp(0u, __float_as_uint(v), CTRL, 0xf, 0xf, true)); }
template <int CTRL> __device__ __forceinline__ unsigned dppu(unsigned v) { return __builtin_amdgcn_update_dpp(0u, v, CTRL, 0xf, 0xf, true); }
__device__ __forceinline__ float sum8(float v) { v += dppf<0xB1>(v); v += dppf<0x4E>(v); v += dppf<0x141>(v); return v; }
__device__ __forceinline__ float sum32(float v) {
    v = sum8(v); v += dppf<0x140>(v);
    const auto r = __builtin_amdgcn_permlane16_swap(__float_as_uint(v), __float_as_uint(v), false, false);
    return __uint_as_float(r[0]) + __uint_as_float(r[1]);
}
__device__ __forceinline__ unsigned max64(unsigned v) {
    v = max(v, dppu<0xB1>(v)); v = max(v, dppu<0x4E>(v)); v = max(v, dppu<0x141>(v)); v = max(v, dppu<0x140>(v));
    auto r = __builtin_amdgcn_permlane16_swap(v, v, false, false); v = max(r[0], r[1]);
    r = __builtin_amdgcn_permlane32_swap(v, v, false, false);
    return max(r[0], r[1]);
}

constexpr int P1_X = 0;
constexpr int P1_W = 64 * 912;
constexpr int P1_WO = P1_W + 57344;
constexpr int P1_ST = P1_WO + 4096;
constexpr int K2_PART = 0;
constexpr int K2_O = 49152;
constexpr int K2_A = 58368;
constexpr int K2_TP = K2_A;
constexpr int K2_R = K2_A + 16384;
constexpr int K2_WOL = K2_R + 128;
constexpr int K2_FW = K2_WOL;
constexpr int K2_FW1 = P1_ST + 1024;
constexpr int K2_WOR = K2_FW1 + 14336;
constexpr int LDS_BYTES = 163840;
constexpr int K2_AL = K2_PART;
constexpr int K2_RED = K2_PART + 8192;
static_assert(K2_WOR + 28672 <= LDS_BYTES && K2_FW + 57344 <= K2_WOR && K2_WOL + 28672 <= K2_FW1, "");
typedef unsigned u32x12 __attribute__((ext_vector_type(12)));
constexpr unsigned SPIN_CAP = 1u << 16;
typedef unsigned u32x8 __attribute__((ext_vector_type(8)));
typedef unsigned u32x16 __attribute__((ext_vector_type(16)));

__device__ __forceinline__ float max16(const f32x16& v) {
    float a = fmaxf(fmaxf(v[0], v[1]), v[2]), b = fmaxf(fmaxf(v[3], v[4]), v[5]);
    a = fmaxf(fmaxf(a, v[6]), v[7]); b = fmaxf(fmaxf(b, v[8]), v[9]);
    a = fmaxf(fmaxf(a, v[10]), v[11]); b = fmaxf(fmaxf(b, v[12]), v[13]);
    a = fmaxf(fmaxf(a, v[14]), v[15]);
    a = fmaxf(a, b);
    return fmaxf(a, __shfl_xor(a, 32));
}

__device__ __forceinline__ void slow_attn(__amdgpu_buffer_rsrc_t rsw, const u16* xrows, const u16* wo, char* smem, int wg, int w, int lane) {
    const int hi = lane >> 5;
    const f32x16 zero = {};
    const unsigned qo = WS_QF + wg * 8192 + w * 1792 + ((lane & 31) >> 3) * 448 + hi * 64 + (lane & 7) * 8;
    const u32x2 qa = __builtin_amdgcn_raw_buffer_load_b64(rsw, qo, 0, SC1), qb = __builtin_amdgcn_raw_buffer_load_b64(rsw, qo + 128, 0, SC1);
    const u32x2 qc = __builtin_amdgcn_raw_buffer_load_b64(rsw, qo + 256, 0, SC1), qd = __builtin_amdgcn_raw_buffer_load_b64(rsw, qo + 384, 0, SC1);
    const u32x4 q0 = {qa[0], qa[1], qb[0], qb[1]};
    u32x4 q1 = {qc[0], qc[1], qd[0], qd[1]};
    if (hi) { q1[2] = 0u; q1[3] = 0u; }
    f32x16 o = zero;
    float m = -INFINITY;
#pragma unroll 1
    for (int t = 0; t < 64; ++t) {
        const unsigned tp = WS_KV + t * 4096 + lane * 16;
        f32x16 s = MFMA32(LDW(tp), q0, zero);
        s = MFMA32(LDW(tp + 1024), q1, s);
        const float mn = fmaxf(m, max16(s));
        const float al = __builtin_amdgcn_exp2f(m - mn);
        m = mn;
#pragma unroll
        for (int i = 0; i < 16; ++i) { s[i] = __builtin_amdgcn_exp2f(s[i] - mn); o[i] *= al; }
        o = SMF(LDW(tp + 2048), LO8(s), o);
        o = SMF(LDW(tp + 3072), HI8(s), o);
    }
    const float rl = 1.f / __shfl(o[12], (lane & 31) + 32);
    char* dst = smem + K2_PART + (lane & 7) * 912 + ((4 * w + ((lane & 31) >> 3)) * 28 + 4 * hi) * 2;
#pragma unroll
    for (int g = 0; g < 4; ++g)
        if (g < 3 || !hi) *(u32x2*)(dst + 16 * g) = (u32x2){pk2(o[4 * g] * rl, o[4 * g + 1] * rl), pk2(o[4 * g + 2] * rl, o[4 * g + 3] * rl)};
    __syncthreads();
    const int g = lane >> 4, c = lane & 15;
#pragma unroll 1
    for (int nt = 0; nt < 7; ++nt) {
        const u32x2 xv = *(const u32x2*)(xrows + (c & 7) * D + 112 * w + 16 * nt + 4 * g);
        f32x4 acc = {blo(xv[0]), bhi(xv[0]), blo(xv[1]), bhi(xv[1])};
#pragma unroll 1
        for (int kk = 0; kk < 14; ++kk) {
            u32x4 af;
#pragma unroll
            for (int e = 0; e < 4; ++e) {
                const u16* wp = wo + (32 * kk + 8 * g + 2 * e) * D + 112 * w + 16 * nt + c;
                af[e] = (unsigned)wp[0] | ((unsigned)wp[D] << 16);
            }
            acc = MFMA16(af, *(const u32x4*)(smem + K2_PART + (c & 7) * 912 + kk * 64 + g * 16), acc);
        }
        if (c < 8) *(f32x4*)(smem + K2_A + c * 1808 + (112 * w + 16 * nt + 4 * g) * 4) = acc;
    }
}

__global__ __launch_bounds__(256, 1) void fused_block(const u16* __restrict__ x, const u16* __restrict__ wq, const u16* __restrict__ wk, const u16* __restrict__ wv,
                                                      const u16* __restrict__ wo, const u16* __restrict__ wr, const u16* __restrict__ wgate, const u16* __restrict__ wup,
                                                      const u16* __restrict__ wdown, char* __restrict__ ws, u16* __restrict__ out) {
    extern __shared__ __attribute__((aligned(16))) char smem[];
    PROF_DECL;
    STAMP(0);
    const int w = __builtin_amdgcn_readfirstlane(threadIdx.x >> 6);
    const int wg = blockIdx.x, tg = wg & 31, cg = wg >> 5;
    const int lane = threadIdx.x & 63;
    unsigned cw;
    {
        unsigned long long pc;
        asm volatile("s_getpc_b64 %0" : "=s"(pc));
        asm volatile("global_load_dword %0, %1, %2" : "=v"(cw) : "v"(min(((wg >> 3) * 8 + (lane & 7)) * 128, TEXT_BYTES - 256)), "s"(pc & ~127ull) : "memory");
    }
    const unsigned lb = (unsigned)(uintptr_t)smem;
    const int r8 = lane >> 3, xo = lane * 16;
    const int p4 = lane & 3, q4 = (lane & 15) >> 2, hi = lane >> 5, blk = (lane >> 4) & 1, tt = w & 1, nt = w >> 1, g = lane >> 4, c = lane & 15, l5 = lane & 31;
    const int mode = cg < 7 ? 0 : 1 + nt, tl = 32 * tt + l5;
    const int tb = min(wg, 90), k0 = tb / 7, jj = tb - k0 * 7, kk = k0 == 12 ? 13 : k0, ng = 4 * jj + w, wq_ = ng / 7;
    u32x16 p1;
    p1[0] = lb + P1_W + (8 * hi + q4) * 128 + ((nt ^ (q4 >> 1)) * 64) + blk * 32 + ((cg == 7 && blk && p4 >= 2) ? (p4 ^ 1) : p4) * 8 + tt * 28672;
    p1[1] = lb + P1_X + tl * 912 + hi * 16 + tt * 448;
    p1[2] = lb + P1_X + (16 * w + c) * 912 + (g ^ (((c + 4) >> 3) & 1)) * 16;
    p1[3] = ((g ^ c) & 1) ? 0u : 0x3F803F80u;
    p1[4] = lb + P1_WO + (8 * g + q4) * 128 + w * 32 + p4 * 8;
    p1[5] = (wg < 91 ? WS_WO + (wq_ * 92 + (kk == 13 ? ng - wq_ * 7 : 8 + ((kk >> 1) * 7 + ng - wq_ * 7) * 2 + (kk & 1))) * 1024 : 0x7FFF0000u) + xo;
    p1[6] = lb + P1_ST + (16 * w + c) * 4;
    p1[7] = lb + P1_ST + 512 + tt * 256 + (32 * nt + 16 * (c & 1) + 4 * g) * 4;
    p1[8] = mode == 0 ? WS_QF + (tg + 32 * (tl >> 3)) * 8192 + (16 * cg + 8 * nt + hi) * 64 + (tl & 7) * 8
                      : WS_KV + (2 * tg + tt) * 4096 + (mode == 1 ? (((l5 & 19) | ((l5 & 4) << 1) | ((l5 & 8) >> 1)) + 32 * hi) * 16 : 2048 + xo);
    p1[9] = p1[0] + (tt ? -28672 : 28672);
    p1[10] = p1[1] + (tt ? -448 : 448);
    p1[11] = 0u;
    p1[12] = lb + P1_ST + (mode < 2 ? tl * 4 : 512 + (32 + l5) * 4);
    p1[13] = lb + P1_ST + (mode < 2 ? 512 + (32 * nt + 4 * hi) * 4 : (32 * tt + 4 * hi) * 4);
    p1[14] = 0u;
    p1[15] = l5 == 28 ? 0x3F803F80u : 0u;
    const int c8 = (lane & 7) ^ ((r8 & 2) << 1), c4 = c8 & 3;
    const int xi = 2 * cg, wi = ((tg >> 3) * 7) >> 1, wst = cg < 7 ? 8 * 896 : 8 * 56;
    int woff = (cg < 7 ? (112 * w + r8) * 896 + cg * 128 + c8 * 16 : (112 * w + r8) * 56 + (c4 < 3 ? c4 * 16 : 40)) + wi * wst;
    int woo = r8 * 896 + (lane & 7) * 16;
    asm volatile("" : "+{v[232:247]}"(p1), "+v"(woff), "+v"(woo));
    SBAR();
    {
        const char* wa = (cg < 7 ? (const char*)wq : (const char*)(c8 < 4 ? wk : wv)) + woff;
        asm volatile("s_mov_b32 s40, %[lw]\n s_mov_b32 s41, %[lx]\n s_mov_b64 s[42:43], %[xb]\n s_mov_b32 s44, 16\n s_mov_b32 s45, %[xi]\n s_mov_b32 s46, %[wi]\n"
            "1:\n s_mov_b32 m0, s41\n s_mov_b32 exec_hi, 0x00ffffff\n global_load_lds_dwordx4 %[xo], s[42:43]\n s_mov_b32 exec_hi, -1\n"
            "s_add_u32 s45, s45, 1\n s_cmp_eq_u32 s45, 16\n s_cselect_b32 s47, -15, 1\n s_mul_i32 s48, s47, 912\n s_add_u32 s41, s41, s48\n"
            "s_mul_i32 s48, s47, 896\n s_ashr_i32 s49, s48, 31\n s_add_u32 s42, s42, s48\n s_addc_u32 s43, s43, s49\n"
            "s_sub_u32 s44, s44, 1\n s_cmp_lt_u32 s44, 2\n s_cbranch_scc1 2f\n"
            "s_mov_b32 m0, s40\n s_add_u32 s46, s46, 1\n global_load_lds_dwordx4 %[wa], off\n"
            "s_cmp_eq_u32 s46, 14\n s_cselect_b32 s47, -13, 1\n s_lshl_b32 s48, s47, 10\n s_add_u32 s40, s40, s48\n"
            "s_mul_i32 s48, s47, %[wst]\n s_ashr_i32 s49, s48, 31\n v_lshl_add_u64 %[wa], %[wa], 0, s[48:49]\n"
            "2:\n s_cmp_lg_u32 s44, 0\n s_cbranch_scc1 1b\n"
            "s_mov_b32 m0, %[lo]\n s_nop 0\n global_load_lds_dwordx4 %[oo], %[ob]\n"
            : [wa] "+v"(wa)
            : [xo] "v"(xo), [xb] "s"((const char*)x + (size_t)(64 * tg + 16 * w + xi) * 896), [lx] "s"(lb + P1_X + (w * 16 + xi) * 912), [lw] "s"(lb + P1_W + w * 14336 + wi * 1024),
              [wst] "s"(wst), [xi] "s"(xi), [wi] "s"(wi), [lo] "s"(lb + P1_WO + w * 1024), [oo] "v"(woo), [ob] "s"((const char*)wo + (32 * kk + 8 * w) * 896 + jj * 128)
            : "s40", "s41", "s42", "s43", "s44", "s45", "s46", "s47", "s48", "s49", "scc", "memory");
    }
    const int t0 = 64 * tg + 8 * cg;
    const bool st1 = wg < 224;
    const int cb1 = (wg & 7) * 28 + (wg >> 3), cb2 = (wg & 7) * 4 + ((wg - 224) >> 3);
    const bool st2 = !st1 && (wg & 7) < 7;
    const int role = __builtin_amdgcn_readfirstlane(st1 ? 1 : st2 ? 2 : 0);
    unsigned tag;
    u32x4 rs;
    rs[0] = __builtin_amdgcn_readfirstlane((unsigned)(uintptr_t)ws);
    rs[1] = __builtin_amdgcn_readfirstlane((unsigned)((uintptr_t)ws >> 32));
    rs[2] = WS_END;
    rs[3] = 0x00020000u;
    asm volatile(
        "s_nop 4\n"
        "s_mov_b32 s40, 2830336\n"
        "buffer_load_dword v249, off, %[rs], s40 sc1\n"
        "s_mov_b32 s42, %[wol]\n"
        "s_mov_b64 s[52:53], %[wop]\n"
        "v_mov_b32 v160, v235\n"
        "v_mov_b32 v161, v235\n"
        "v_mov_b32 v162, v235\n"
        "v_mov_b32 v163, v235\n"
        "v_mov_b32 v156, 0x3f803f80\n"
        "v_mov_b32 v157, 0x3f803f80\n"
        "v_mov_b32 v158, 0x3f803f80\n"
        "v_mov_b32 v159, 0x3f803f80\n"
        "s_mov_b32 s44, 0\n"
        "s_mov_b32 s45, -1\n"
        "s_mov_b32 s46, 0x22222222\n"
        "s_mov_b32 s47, 0x22222222\n"
        "s_mov_b32 s48, 0x44444444\n"
        "s_mov_b32 s49, 0x44444444\n"
        "s_mov_b32 s50, 0x88888888\n"
        "s_mov_b32 s51, 0x88888888\n"
        "s_waitcnt vmcnt(1)\n"
        "s_barrier\n"
        "global_load_dword v248, %[pf], %[pfb]\n"
        PROF_STAMP_ASM(1)
        "ds_read_b64_tr_b16 v[176:177], v236\n"
        "ds_read_b64_tr_b16 v[178:179], v236 offset:512\n"
        "s_cmp_eq_u32 %[mode], 2\n"
        "s_waitcnt lgkmcnt(0)\n"
        "buffer_store_dwordx4 v[176:179], v237, %[rs], 0 offen sc1\n"
        "s_cbranch_scc1 .Lp1v%=\n"
        "ds_read_b64_tr_b16 v[40:41], v232 offset:0\n"
        "ds_read_b64_tr_b16 v[42:43], v232 offset:512\n"
        "ds_read_b128 v[72:75], v233 offset:0\n"
        "ds_read_b64_tr_b16 v[44:45], v232 offset:2048\n"
        "ds_read_b64_tr_b16 v[46:47], v232 offset:2560\n"
        "ds_read_b128 v[76:79], v233 offset:32\n"
        "ds_read_b128 v[104:107], v234 offset:0\n"
        "ds_read_b64_tr_b16 v[48:49], v232 offset:4096\n"
        "ds_read_b64_tr_b16 v[50:51], v232 offset:4608\n"
        "ds_read_b128 v[80:83], v233 offset:64\n"
        "ds_read_b64_tr_b16 v[52:53], v232 offset:6144\n"
        "ds_read_b64_tr_b16 v[54:55], v232 offset:6656\n"
        "ds_read_b128 v[84:87], v233 offset:96\n"
        "s_waitcnt lgkmcnt(10)\n"
        "v_mfma_f32_32x32x16_bf16 v[128:143], v[40:43], v[72:75], 0\n"
        "ds_read_b128 v[108:111], v234 offset:64\n"
        "ds_read_b64_tr_b16 v[56:57], v232 offset:8192\n"
        "ds_read_b64_tr_b16 v[58:59], v232 offset:8704\n"
        "v_mfma_f32_16x16x32_bf16 v[152:155], v[40:43], v[160:163], 0\n"
        "s_waitcnt lgkmcnt(10)\n"
        "v_mfma_f32_32x32x16_bf16 v[128:143], v[44:47], v[76:79], v[128:143]\n"
        "ds_read_b128 v[88:91], v233 offset:128\n"
        "ds_read_b64_tr_b16 v[60:61], v232 offset:10240\n"
        "ds_read_b64_tr_b16 v[62:63], v232 offset:10752\n"
        "v_mfma_f32_16x16x32_bf16 v[152:155], v[44:47], v[160:163], v[152:155]\n"
        "s_waitcnt lgkmcnt(12)\n"
        "v_mfma_f32_16x16x32_bf16 v[144:147], v[104:107], v[104:107], 0\n"
        "ds_read_b128 v[92:95], v233 offset:160\n"
        "v_mfma_f32_16x16x32_bf16 v[148:151], v[156:159], v[104:107], 0\n"
        "s_waitcnt lgkmcnt(10)\n"
        "v_mfma_f32_32x32x16_bf16 v[128:143], v[48:51], v[80:83], v[128:143]\n"
        "ds_read_b128 v[112:115], v234 offset:128\n"
        "ds_read_b64_tr_b16 v[64:65], v232 offset:12288\n"
        "ds_read_b64_tr_b16 v[66:67], v232 offset:12800\n"
        "v_mfma_f32_16x16x32_bf16 v[152:155], v[48:51], v[160:163], v[152:155]\n"
        "s_waitcnt lgkmcnt(10)\n"
        "v_mfma_f32_32x32x16_bf16 v[128:143], v[52:55], v[84:87], v[128:143]\n"
        "ds_read_b128 v[96:99], v233 offset:192\n"
        "ds_read_b64_tr_b16 v[68:69], v232 offset:14336\n"
        "ds_read_b64_tr_b16 v[70:71], v232 offset:14848\n"
        "s_mov_b32 m0, s42\n"
        "s_add_u32 s42, s42, 0x1000\n"
        "global_load_lds_dwordx4 %[xo], s[52:53] offset:0\n"
        "global_load_lds_dwordx4 %[xo], s[52:53] offset:1024\n"
        "global_load_lds_dwordx4 %[xo], s[52:53] offset:2048\n"
        "global_load_lds_dwordx4 %[xo], s[52:53] offset:3072\n"
        "s_add_u32 s52, s52, 0x1000\n"
        "s_addc_u32 s53, s53, 0\n"
        "v_mfma_f32_16x16x32_bf16 v[152:155], v[52:55], v[160:163], v[152:155]\n"
        "s_waitcnt lgkmcnt(12)\n"
        "v_mfma_f32_16x16x32_bf16 v[144:147], v[108:111], v[108:111], v[144:147]\n"
        "ds_read_b128 v[100:103], v233 offset:224\n"
        "v_mfma_f32_16x16x32_bf16 v[148:151], v[156:159], v[108:111], v[148:151]\n"
        "s_waitcnt lgkmcnt(10)\n"
        "v_mfma_f32_32x32x16_bf16 v[128:143], v[56:59], v[88:91], v[128:143]\n"
        "ds_read_b128 v[116:119], v234 offset:192\n"
        "ds_read_b64_tr_b16 v[40:41], v232 offset:16384\n"
        "ds_read_b64_tr_b16 v[42:43], v232 offset:16896\n"
        "v_mfma_f32_16x16x32_bf16 v[152:155], v[56:59], v[160:163], v[152:155]\n"
        "s_waitcnt lgkmcnt(10)\n"
        "v_mfma_f32_32x32x16_bf16 v[128:143], v[60:63], v[92:95], v[128:143]\n"
        "ds_read_b128 v[72:75], v233 offset:256\n"
        "ds_read_b64_tr_b16 v[44:45], v232 offset:18432\n"
        "ds_read_b64_tr_b16 v[46:47], v232 offset:18944\n"
        "v_mfma_f32_16x16x32_bf16 v[152:155], v[60:63], v[160:163], v[152:155]\n"
        "s_waitcnt lgkmcnt(12)\n"
        "v_mfma_f32_16x16x32_bf16 v[144:147], v[112:115], v[112:115], v[144:147]\n"
        "ds_read_b128 v[76:79], v233 offset:288\n"
        "v_mfma_f32_16x16x32_bf16 v[148:151], v[156:159], v[112:115], v[148:151]\n"
        "s_waitcnt lgkmcnt(10)\n"
        "v_mfma_f32_32x32x16_bf16 v[128:143], v[64:67], v[96:99], v[128:143]\n"
        "ds_read_b128 v[104:107], v234 offset:256\n"
        "ds_read_b64_tr_b16 v[48:49], v232 offset:20480\n"
        "ds_read_b64_tr_b16 v[50:51], v232 offset:20992\n"
        "v_mfma_f32_16x16x32_bf16 v[152:155], v[64:67], v[160:163], v[152:155]\n"
        "s_waitcnt lgkmcnt(10)\n"
        "v_mfma_f32_32x32x16_bf16 v[128:143], v[68:71], v[100:103], v[128:143]\n"
        "ds_read_b128 v[80:83], v233 offset:320\n"
        "ds_read_b64_tr_b16 v[52:53], v232 offset:22528\n"
        "ds_read_b64_tr_b16 v[54:55], v232 offset:23040\n"
        "s_mov_b32 m0, s42\n"
        "s_add_u32 s42, s42, 0x1000\n"
        "global_load_lds_dwordx4 %[xo], s[52:53] offset:0\n"
        "global_load_lds_dwordx4 %[xo], s[52:53] offset:1024\n"
        "global_load_lds_dwordx4 %[xo], s[52:53] offset:2048\n"
        "s_add_u32 s52, s52, 0x1000\n"
        "s_addc_u32 s53, s53, 0\n"
        "v_mfma_f32_16x16x32_bf16 v[152:155], v[68:71], v[160:163], v[152:155]\n"
        "s_waitcnt lgkmcnt(12)\n"
        "v_mfma_f32_16x16x32_bf16 v[144:147], v[116:119], v[116:119], v[144:147]\n"
        "ds_read_b128 v[84:87], v233 offset:352\n"
        "v_mfma_f32_16x16x32_bf16 v[148:151], v[156:159], v[116:119], v[148:151]\n"
        "s_waitcnt lgkmcnt(10)\n"
        "v_mfma_f32_32x32x16_bf16 v[128:143], v[40:43], v[72:75], v[128:143]\n"
        "ds_read_b128 v[108:111], v234 offset:320\n"
        "ds_read_b64_tr_b16 v[56:57], v232 offset:24576\n"
        "ds_read_b64_tr_b16 v[58:59], v232 offset:25088\n"
        "v_mfma_f32_16x16x32_bf16 v[152:155], v[40:43], v[160:163], v[152:155]\n"
        "s_waitcnt lgkmcnt(10)\n"
        "v_mfma_f32_32x32x16_bf16 v[128:143], v[44:47], v[76:79], v[128:143]\n"
        "ds_read_b128 v[88:91], v233 offset:384\n"
        "ds_read_b64_tr_b16 v[60:61], v232 offset:26624\n"
        "ds_read_b64_tr_b16 v[62:63], v232 offset:27136\n"
        "v_mfma_f32_16x16x32_bf16 v[152:155], v[44:47], v[160:163], v[152:155]\n"
        "s_waitcnt lgkmcnt(12)\n"
        "v_mfma_f32_16x16x32_bf16 v[144:147], v[104:107], v[104:107], v[144:147]\n"
        "ds_read_b128 v[92:95], v233 offset:416\n"
        "v_mfma_f32_16x16x32_bf16 v[148:151], v[156:159], v[104:107], v[148:151]\n"
        "s_waitcnt lgkmcnt(10)\n"
        "v_mfma_f32_32x32x16_bf16 v[128:143], v[48:51], v[80:83], v[128:143]\n"
        "ds_read_b128 v[112:115], v234 offset:384\n"
        "ds_read_b64_tr_b16 v[64:65], v241 offset:0\n"
        "ds_read_b64_tr_b16 v[66:67], v241 offset:512\n"
        "v_mfma_f32_16x16x32_bf16 v[152:155], v[48:51], v[160:163], v[152:155]\n"
        "s_waitcnt lgkmcnt(10)\n"
        "v_mfma_f32_32x32x16_bf16 v[128:143], v[52:55], v[84:87], v[128:143]\n"
        "ds_read_b128 v[96:99], v242 offset:0\n"
        "ds_read_b128 v[116:119], v234 offset:448\n"
        "ds_read_b64_tr_b16 v[68:69], v241 offset:2048\n"
        "v_mfma_f32_16x16x32_bf16 v[152:155], v[52:55], v[160:163], v[152:155]\n"
        "s_waitcnt lgkmcnt(12)\n"
        "v_mfma_f32_16x16x32_bf16 v[144:147], v[108:111], v[108:111], v[144:147]\n"
        "ds_read_b64_tr_b16 v[70:71], v241 offset:2560\n"
        "v_mfma_f32_16x16x32_bf16 v[148:151], v[156:159], v[108:111], v[148:151]\n"
        "s_waitcnt lgkmcnt(10)\n"
        "v_mfma_f32_32x32x16_bf16 v[128:143], v[56:59], v[88:91], v[128:143]\n"
        "ds_read_b128 v[100:103], v242 offset:32\n"
        "ds_read_b128 v[104:107], v234 offset:512\n"
        "ds_read_b64_tr_b16 v[40:41], v241 offset:4096\n"
        "v_mfma_f32_16x16x32_bf16 v[152:155], v[56:59], v[160:163], v[152:155]\n"
        "s_waitcnt lgkmcnt(10)\n"
        "v_mfma_f32_32x32x16_bf16 v[128:143], v[60:63], v[92:95], v[128:143]\n"
        "ds_read_b64_tr_b16 v[42:43], v241 offset:4608\n"
        "ds_read_b128 v[72:75], v242 offset:64\n"
        "ds_read_b128 v[108:111], v234 offset:576\n"
        "v_mfma_f32_16x16x32_bf16 v[152:155], v[60:63], v[160:163], v[152:155]\n"
        "s_waitcnt lgkmcnt(12)\n"
        "v_mfma_f32_16x16x32_bf16 v[144:147], v[112:115], v[112:115], v[144:147]\n"
        "ds_read_b64_tr_b16 v[44:45], v241 offset:6144\n"
        "v_mfma_f32_16x16x32_bf16 v[148:151], v[156:159], v[112:115], v[148:151]\n"
        "s_waitcnt lgkmcnt(10)\n"
        "v_mfma_f32_32x32x16_bf16 v[128:143], v[64:67], v[96:99], v[128:143]\n"
        "ds_read_b64_tr_b16 v[46:47], v241 offset:6656\n"
        "ds_read_b128 v[76:79], v242 offset:96\n"
        "ds_read_b128 v[112:115], v234 offset:640\n"
        "s_waitcnt lgkmcnt(12)\n"
        "v_mfma_f32_16x16x32_bf16 v[144:147], v[116:119], v[116:119], v[144:147]\n"
        "ds_read_b64_tr_b16 v[48:49], v241 offset:8192\n"
        "v_mfma_f32_16x16x32_bf16 v[148:151], v[156:159], v[116:119], v[148:151]\n"
        "s_waitcnt lgkmcnt(10)\n"
        "v_mfma_f32_32x32x16_bf16 v[128:143], v[68:71], v[100:103], v[128:143]\n"
        "ds_read_b64_tr_b16 v[50:51], v241 offset:8704\n"
        "ds_read_b128 v[80:83], v242 offset:128\n"
        "ds_read_b128 v[116:119], v234 offset:704\n"
        "s_waitcnt lgkmcnt(12)\n"
        "v_mfma_f32_16x16x32_bf16 v[144:147], v[104:107], v[104:107], v[144:147]\n"
        "ds_read_b64_tr_b16 v[52:53], v241 offset:10240\n"
        "v_mfma_f32_16x16x32_bf16 v[148:151], v[156:159], v[104:107], v[148:151]\n"
        "s_waitcnt lgkmcnt(10)\n"
        "v_mfma_f32_32x32x16_bf16 v[128:143], v[40:43], v[72:75], v[128:143]\n"
        "ds_read_b64_tr_b16 v[54:55], v241 offset:10752\n"
        "ds_read_b128 v[84:87], v242 offset:160\n"
        "ds_read_b128 v[104:107], v234 offset:768\n"
        "s_waitcnt lgkmcnt(12)\n"
        "v_mfma_f32_16x16x32_bf16 v[144:147], v[108:111], v[108:111], v[144:147]\n"
        "ds_read_b64_tr_b16 v[56:57], v241 offset:12288\n"
        "v_mfma_f32_16x16x32_bf16 v[148:151], v[156:159], v[108:111], v[148:151]\n"
        "s_waitcnt lgkmcnt(10)\n"
        "v_mfma_f32_32x32x16_bf16 v[128:143], v[44:47], v[76:79], v[128:143]\n"
        "ds_read_b64_tr_b16 v[58:59], v241 offset:12800\n"
        "ds_read_b128 v[88:91], v242 offset:192\n"
        "ds_read_b128 v[108:111], v234 offset:832\n"
        "s_waitcnt lgkmcnt(12)\n"
        "v_mfma_f32_16x16x32_bf16 v[144:147], v[112:115], v[112:115], v[144:147]\n"
        "ds_read_b64_tr_b16 v[60:61], v241 offset:14336\n"
        "v_mfma_f32_16x16x32_bf16 v[148:151], v[156:159], v[112:115], v[148:151]\n"
        "s_waitcnt lgkmcnt(10)\n"
        "v_mfma_f32_32x32x16_bf16 v[128:143], v[48:51], v[80:83], v[128:143]\n"
        "ds_read_b64_tr_b16 v[62:63], v241 offset:14848\n"
        "ds_read_b128 v[92:95], v242 offset:224\n"
        "ds_read_b64_tr_b16 v[64:65], v241 offset:16384\n"
        "s_waitcnt lgkmcnt(12)\n"
        "v_mfma_f32_16x16x32_bf16 v[144:147], v[116:119], v[116:119], v[144:147]\n"
        "ds_read_b64_tr_b16 v[66:67], v241 offset:16896\n"
        "v_mfma_f32_16x16x32_bf16 v[148:151], v[156:159], v[116:119], v[148:151]\n"
        "s_waitcnt lgkmcnt(10)\n"
        "v_mfma_f32_32x32x16_bf16 v[128:143], v[52:55], v[84:87], v[128:143]\n"
        "ds_read_b128 v[96:99], v242 offset:256\n"
        "ds_read_b64_tr_b16 v[68:69], v241 offset:18432\n"
        "ds_read_b64_tr_b16 v[70:71], v241 offset:18944\n"
        "s_waitcnt lgkmcnt(12)\n"
        "v_mfma_f32_16x16x32_bf16 v[144:147], v[104:107], v[104:107], v[144:147]\n"
        "ds_read_b128 v[100:103], v242 offset:288\n"
        "v_mfma_f32_16x16x32_bf16 v[148:151], v[156:159], v[104:107], v[148:151]\n"
        "s_waitcnt lgkmcnt(10)\n"
        "v_mfma_f32_32x32x16_bf16 v[128:143], v[56:59], v[88:91], v[128:143]\n"
        "ds_read_b64_tr_b16 v[40:41], v241 offset:20480\n"
        "ds_read_b64_tr_b16 v[42:43], v241 offset:20992\n"
        "ds_read_b128 v[72:75], v242 offset:320\n"
        "s_waitcnt lgkmcnt(12)\n"
        "v_mfma_f32_16x16x32_bf16 v[144:147], v[108:111], v[108:111], v[144:147]\n"
        "ds_read_b64_tr_b16 v[44:45], v241 offset:22528\n"
        "v_mfma_f32_16x16x32_bf16 v[148:151], v[156:159], v[108:111], v[148:151]\n"
        "s_waitcnt lgkmcnt(10)\n"
        "v_mfma_f32_32x32x16_bf16 v[128:143], v[60:63], v[92:95], v[128:143]\n"
        "ds_read_b64_tr_b16 v[46:47], v241 offset:23040\n"
        "ds_read_b128 v[76:79], v242 offset:352\n"
        "ds_read_b64_tr_b16 v[48:49], v241 offset:24576\n"
        "s_waitcnt lgkmcnt(10)\n"
        "v_mfma_f32_32x32x16_bf16 v[128:143], v[64:67], v[96:99], v[128:143]\n"
        "ds_read_b64_tr_b16 v[50:51], v241 offset:25088\n"
        "ds_read_b128 v[80:83], v242 offset:384\n"
        "ds_read_b64_tr_b16 v[52:53], v241 offset:26624\n"
        "s_waitcnt lgkmcnt(10)\n"
        "v_mfma_f32_32x32x16_bf16 v[128:143], v[68:71], v[100:103], v[128:143]\n"
        "ds_read_b64_tr_b16 v[54:55], v241 offset:27136\n"
        "ds_read_b128 v[84:87], v242 offset:416\n"
        "s_nop 3\n"
        "v_cndmask_b32 v180, v144, v145, s[46:47]\n"
        "v_cndmask_b32 v180, v180, v146, s[48:49]\n"
        "v_cndmask_b32 v180, v180, v147, s[50:51]\n"
        "v_mul_f32 v181, 0x3b124925, v148\n"
        "s_waitcnt lgkmcnt(9)\n"
        "v_mfma_f32_32x32x16_bf16 v[128:143], v[40:43], v[72:75], v[128:143]\n"
        "v_mul_f32 v180, 0x3b124925, v180\n"
        "v_fma_f32 v180, -v181, v181, v180\n"
        "v_max_f32 v180, 0, v180\n"
        "v_cvt_pk_bf16_f32 v180, v180, v181\n"
        "v_and_b32 v181, 0xffff0000, v180\n"
        "s_waitcnt lgkmcnt(6)\n"
        "v_mfma_f32_32x32x16_bf16 v[128:143], v[44:47], v[76:79], v[128:143]\n"
        "v_lshlrev_b32 v180, 16, v180\n"
        "v_add_f32 v180, 0x3727c5ac, v180\n"
        "v_cvt_pk_bf16_f32 v180, v180, v180\n"
        "v_lshlrev_b32 v180, 16, v180\n"
        "v_sqrt_f32 v180, v180\n"
        "s_waitcnt lgkmcnt(3)\n"
        "v_mfma_f32_32x32x16_bf16 v[128:143], v[48:51], v[80:83], v[128:143]\n"
        "s_nop 0\n"
        "v_cvt_pk_bf16_f32 v180, v180, v180\n"
        "v_lshlrev_b32 v180, 16, v180\n"
        "v_rcp_f32 v180, v180\n"
        "s_nop 0\n"
        "s_waitcnt lgkmcnt(0)\n"
        "v_mfma_f32_32x32x16_bf16 v[128:143], v[52:55], v[84:87], v[128:143]\n"
        "s_branch .Lp1j%=\n"
        ".Lp1v%=:\n"
        "ds_read_b64_tr_b16 v[40:41], v232 offset:0\n"
        "ds_read_b64_tr_b16 v[42:43], v232 offset:512\n"
        "ds_read_b128 v[72:75], v233 offset:0\n"
        "ds_read_b64_tr_b16 v[44:45], v232 offset:2048\n"
        "ds_read_b64_tr_b16 v[46:47], v232 offset:2560\n"
        "ds_read_b128 v[76:79], v233 offset:32\n"
        "ds_read_b128 v[104:107], v234 offset:0\n"
        "ds_read_b64_tr_b16 v[48:49], v232 offset:4096\n"
        "ds_read_b64_tr_b16 v[50:51], v232 offset:4608\n"
        "ds_read_b128 v[80:83], v233 offset:64\n"
        "ds_read_b64_tr_b16 v[52:53], v232 offset:6144\n"
        "ds_read_b64_tr_b16 v[54:55], v232 offset:6656\n"
        "ds_read_b128 v[84:87], v233 offset:96\n"
        "s_waitcnt lgkmcnt(10)\n"
        "v_mfma_f32_32x32x16_bf16 v[128:143], v[72:75], v[40:43], 0\n"
        "ds_read_b128 v[108:111], v234 offset:64\n"
        "ds_read_b64_tr_b16 v[56:57], v232 offset:8192\n"
        "ds_read_b64_tr_b16 v[58:59], v232 offset:8704\n"
        "v_mfma_f32_16x16x32_bf16 v[152:155], v[40:43], v[160:163], 0\n"
        "s_waitcnt lgkmcnt(10)\n"
        "v_mfma_f32_32x32x16_bf16 v[128:143], v[76:79], v[44:47], v[128:143]\n"
        "ds_read_b128 v[88:91], v233 offset:128\n"
        "ds_read_b64_tr_b16 v[60:61], v232 offset:10240\n"
        "ds_read_b64_tr_b16 v[62:63], v232 offset:10752\n"
        "v_mfma_f32_16x16x32_bf16 v[152:155], v[44:47], v[160:163], v[152:155]\n"
        "s_waitcnt lgkmcnt(12)\n"
        "v_mfma_f32_16x16x32_bf16 v[144:147], v[104:107], v[104:107], 0\n"
        "ds_read_b128 v[92:95], v233 offset:160\n"
        "v_mfma_f32_16x16x32_bf16 v[148:151], v[156:159], v[104:107], 0\n"
        "s_waitcnt lgkmcnt(10)\n"
        "v_mfma_f32_32x32x16_bf16 v[128:143], v[80:83], v[48:51], v[128:143]\n"
        "ds_read_b128 v[112:115], v234 offset:128\n"
        "ds_read_b64_tr_b16 v[64:65], v232 offset:12288\n"
        "ds_read_b64_tr_b16 v[66:67], v232 offset:12800\n"
        "v_mfma_f32_16x16x32_bf16 v[152:155], v[48:51], v[160:163], v[152:155]\n"
        "s_waitcnt lgkmcnt(10)\n"
        "v_mfma_f32_32x32x16_bf16 v[128:143], v[84:87], v[52:55], v[128:143]\n"
        "ds_read_b128 v[96:99], v233 offset:192\n"
        "ds_read_b64_tr_b16 v[68:69], v232 offset:14336\n"
        "ds_read_b64_tr_b16 v[70:71], v232 offset:14848\n"
        "s_mov_b32 m0, s42\n"
        "s_add_u32 s42, s42, 0x1000\n"
        "global_load_lds_dwordx4 %[xo], s[52:53] offset:0\n"
        "global_load_lds_dwordx4 %[xo], s[52:53] offset:1024\n"
        "global_load_lds_dwordx4 %[xo], s[52:53] offset:2048\n"
        "global_load_lds_dwordx4 %[xo], s[52:53] offset:3072\n"
        "s_add_u32 s52, s52, 0x1000\n"
        "s_addc_u32 s53, s53, 0\n"
        "v_mfma_f32_16x16x32_bf16 v[152:155], v[52:55], v[160:163], v[152:155]\n"
        "s_waitcnt lgkmcnt(12)\n"
        "v_mfma_f32_16x16x32_bf16 v[144:147], v[108:111], v[108:111], v[144:147]\n"
        "ds_read_b128 v[100:103], v233 offset:224\n"
        "v_mfma_f32_16x16x32_bf16 v[148:151], v[156:159], v[108:111], v[148:151]\n"
        "s_waitcnt lgkmcnt(10)\n"
        "v_mfma_f32_32x32x16_bf16 v[128:143], v[88:91], v[56:59], v[128:143]\n"
        "ds_read_b128 v[116:119], v234 offset:192\n"
        "ds_read_b64_tr_b16 v[40:41], v232 offset:16384\n"
        "ds_read_b64_tr_b16 v[42:43], v232 offset:16896\n"
        "v_mfma_f32_16x16x32_bf16 v[152:155], v[56:59], v[160:163], v[152:155]\n"
        "s_waitcnt lgkmcnt(10)\n"
        "v_mfma_f32_32x32x16_bf16 v[128:143], v[92:95], v[60:63], v[128:143]\n"
        "ds_read_b128 v[72:75], v233 offset:256\n"
        "ds_read_b64_tr_b16 v[44:45], v232 offset:18432\n"
        "ds_read_b64_tr_b16 v[46:47], v232 offset:18944\n"
        "v_mfma_f32_16x16x32_bf16 v[152:155], v[60:63], v[160:163], v[152:155]\n"
        "s_waitcnt lgkmcnt(12)\n"
        "v_mfma_f32_16x16x32_bf16 v[144:147], v[112:115], v[112:115], v[144:147]\n"
        "ds_read_b128 v[76:79], v233 offset:288\n"
        "v_mfma_f32_16x16x32_bf16 v[148:151], v[156:159], v[112:115], v[148:151]\n"
        "s_waitcnt lgkmcnt(10)\n"
        "v_mfma_f32_32x32x16_bf16 v[128:143], v[96:99], v[64:67], v[128:143]\n"
        "ds_read_b128 v[104:107], v234 offset:256\n"
        "ds_read_b64_tr_b16 v[48:49], v232 offset:20480\n"
        "ds_read_b64_tr_b16 v[50:51], v232 offset:20992\n"
        "v_mfma_f32_16x16x32_bf16 v[152:155], v[64:67], v[160:163], v[152:155]\n"
        "s_waitcnt lgkmcnt(10)\n"
        "v_mfma_f32_32x32x16_bf16 v[128:143], v[100:103], v[68:71], v[128:143]\n"
        "ds_read_b128 v[80:83], v233 offset:320\n"
        "ds_read_b64_tr_b16 v[52:53], v232 offset:22528\n"
        "ds_read_b64_tr_b16 v[54:55], v232 offset:23040\n"
        "s_mov_b32 m0, s42\n"
        "s_add_u32 s42, s42, 0x1000\n"
        "global_load_lds_dwordx4 %[xo], s[52:53] offset:0\n"
        "global_load_lds_dwordx4 %[xo], s[52:53] offset:1024\n"
        "global_load_lds_dwordx4 %[xo], s[52:53] offset:2048\n"
        "s_add_u32 s52, s52, 0x1000\n"
        "s_addc_u32 s53, s53, 0\n"
        "v_mfma_f32_16x16x32_bf16 v[152:155], v[68:71], v[160:163], v[152:155]\n"
        "s_waitcnt lgkmcnt(12)\n"
        "v_mfma_f32_16x16x32_bf16 v[144:147], v[116:119], v[116:119], v[144:147]\n"
        "ds_read_b128 v[84:87], v233 offset:352\n"
        "v_mfma_f32_16x16x32_bf16 v[148:151], v[156:159], v[116:119], v[148:151]\n"
        "s_waitcnt lgkmcnt(10)\n"
        "v_mfma_f32_32x32x16_bf16 v[128:143], v[72:75], v[40:43], v[128:143]\n"
        "ds_read_b128 v[108:111], v234 offset:320\n"
        "ds_read_b64_tr_b16 v[56:57], v232 offset:24576\n"
        "ds_read_b64_tr_b16 v[58:59], v232 offset:25088\n"
        "v_mfma_f32_16x16x32_bf16 v[152:155], v[40:43], v[160:163], v[152:155]\n"
        "s_waitcnt lgkmcnt(10)\n"
        "v_mfma_f32_32x32x16_bf16 v[128:143], v[76:79], v[44:47], v[128:143]\n"
        "ds_read_b128 v[88:91], v233 offset:384\n"
        "ds_read_b64_tr_b16 v[60:61], v232 offset:26624\n"
        "ds_read_b64_tr_b16 v[62:63], v232 offset:27136\n"
        "v_mfma_f32_16x16x32_bf16 v[152:155], v[44:47], v[160:163], v[152:155]\n"
        "s_waitcnt lgkmcnt(12)\n"
        "v_mfma_f32_16x16x32_bf16 v[144:147], v[104:107], v[104:107], v[144:147]\n"
        "ds_read_b128 v[92:95], v233 offset:416\n"
        "v_mfma_f32_16x16x32_bf16 v[148:151], v[156:159], v[104:107], v[148:151]\n"
        "s_waitcnt lgkmcnt(10)\n"
        "v_mfma_f32_32x32x16_bf16 v[128:143], v[80:83], v[48:51], v[128:143]\n"
        "ds_read_b128 v[112:115], v234 offset:384\n"
        "ds_read_b64_tr_b16 v[64:65], v241 offset:0\n"
        "ds_read_b64_tr_b16 v[66:67], v241 offset:512\n"
        "v_mfma_f32_16x16x32_bf16 v[152:155], v[48:51], v[160:163], v[152:155]\n"
        "s_waitcnt lgkmcnt(10)\n"
        "v_mfma_f32_32x32x16_bf16 v[128:143], v[84:87], v[52:55], v[128:143]\n"
        "ds_read_b128 v[96:99], v242 offset:0\n"
        "ds_read_b128 v[116:119], v234 offset:448\n"
        "ds_read_b64_tr_b16 v[68:69], v241 offset:2048\n"
        "v_mfma_f32_16x16x32_bf16 v[152:155], v[52:55], v[160:163], v[152:155]\n"
        "s_waitcnt lgkmcnt(12)\n"
        "v_mfma_f32_16x16x32_bf16 v[144:147], v[108:111], v[108:111], v[144:147]\n"
        "ds_read_b64_tr_b16 v[70:71], v241 offset:2560\n"
        "v_mfma_f32_16x16x32_bf16 v[148:151], v[156:159], v[108:111], v[148:151]\n"
        "s_waitcnt lgkmcnt(10)\n"
        "v_mfma_f32_32x32x16_bf16 v[128:143], v[88:91], v[56:59], v[128:143]\n"
        "ds_read_b128 v[100:103], v242 offset:32\n"
        "ds_read_b128 v[104:107], v234 offset:512\n"
        "ds_read_b64_tr_b16 v[40:41], v241 offset:4096\n"
        "v_mfma_f32_16x16x32_bf16 v[152:155], v[56:59], v[160:163], v[152:155]\n"
        "s_waitcnt lgkmcnt(10)\n"
        "v_mfma_f32_32x32x16_bf16 v[128:143], v[92:95], v[60:63], v[128:143]\n"
        "ds_read_b64_tr_b16 v[42:43], v241 offset:4608\n"
        "ds_read_b128 v[72:75], v242 offset:64\n"
        "ds_read_b128 v[108:111], v234 offset:576\n"
        "v_mfma_f32_16x16x32_bf16 v[152:155], v[60:63], v[160:163], v[152:155]\n"
        "s_waitcnt lgkmcnt(12)\n"
        "v_mfma_f32_16x16x32_bf16 v[144:147], v[112:115], v[112:115], v[144:147]\n"
        "ds_read_b64_tr_b16 v[44:45], v241 offset:6144\n"
        "v_mfma_f32_16x16x32_bf16 v[148:151], v[156:159], v[112:115], v[148:151]\n"
        "s_waitcnt lgkmcnt(10)\n"
        "v_mfma_f32_32x32x16_bf16 v[128:143], v[96:99], v[64:67], v[128:143]\n"
        "ds_read_b64_tr_b16 v[46:47], v241 offset:6656\n"
        "ds_read_b128 v[76:79], v242 offset:96\n"
        "ds_read_b128 v[112:115], v234 offset:640\n"
        "s_waitcnt lgkmcnt(12)\n"
        "v_mfma_f32_16x16x32_bf16 v[144:147], v[116:119], v[116:119], v[144:147]\n"
        "ds_read_b64_tr_b16 v[48:49], v241 offset:8192\n"
        "v_mfma_f32_16x16x32_bf16 v[148:151], v[156:159], v[116:119], v[148:151]\n"
        "s_waitcnt lgkmcnt(10)\n"
        "v_mfma_f32_32x32x16_bf16 v[128:143], v[100:103], v[68:71], v[128:143]\n"
        "ds_read_b64_tr_b16 v[50:51], v241 offset:8704\n"
        "ds_read_b128 v[80:83], v242 offset:128\n"
        "ds_read_b128 v[116:119], v234 offset:704\n"
        "s_waitcnt lgkmcnt(12)\n"
        "v_mfma_f32_16x16x32_bf16 v[144:147], v[104:107], v[104:107], v[144:147]\n"
        "ds_read_b64_tr_b16 v[52:53], v241 offset:10240\n"
        "v_mfma_f32_16x16x32_bf16 v[148:151], v[156:159], v[104:107], v[148:151]\n"
        "s_waitcnt lgkmcnt(10)\n"
        "v_mfma_f32_32x32x16_bf16 v[128:143], v[72:75], v[40:43], v[128:143]\n"
        "ds_read_b64_tr_b16 v[54:55], v241 offset:10752\n"
        "ds_read_b128 v[84:87], v242 offset:160\n"
        "ds_read_b128 v[104:107], v234 offset:768\n"
        "s_waitcnt lgkmcnt(12)\n"
        "v_mfma_f32_16x16x32_bf16 v[144:147], v[108:111], v[108:111], v[144:147]\n"
        "ds_read_b64_tr_b16 v[56:57], v241 offset:12288\n"
        "v_mfma_f32_16x16x32_bf16 v[148:151], v[156:159], v[108:111], v[148:151]\n"
        "s_waitcnt lgkmcnt(10)\n"
        "v_mfma_f32_32x32x16_bf16 v[128:143], v[76:79], v[44:47], v[128:143]\n"
        "ds_read_b64_tr_b16 v[58:59], v241 offset:12800\n"
        "ds_read_b128 v[88:91], v242 offset:192\n"
        "ds_read_b128 v[108:111], v234 offset:832\n"
        "s_waitcnt lgkmcnt(12)\n"
        "v_mfma_f32_16x16x32_bf16 v[144:147], v[112:115], v[112:115], v[144:147]\n"
        "ds_read_b64_tr_b16 v[60:61], v241 offset:14336\n"
        "v_mfma_f32_16x16x32_bf16 v[148:151], v[156:159], v[112:115], v[148:151]\n"
        "s_waitcnt lgkmcnt(10)\n"
        "v_mfma_f32_32x32x16_bf16 v[128:143], v[80:83], v[48:51], v[128:143]\n"
        "ds_read_b64_tr_b16 v[62:63], v241 offset:14848\n"
        "ds_read_b128 v[92:95], v242 offset:224\n"
        "ds_read_b64_tr_b16 v[64:65], v241 offset:16384\n"
        "s_waitcnt lgkmcnt(12)\n"
        "v_mfma_f32_16x16x32_bf16 v[144:147], v[116:119], v[116:119], v[144:147]\n"
        "ds_read_b64_tr_b16 v[66:67], v241 offset:16896\n"
        "v_mfma_f32_16x16x32_bf16 v[148:151], v[156:159], v[116:119], v[148:151]\n"
        "s_waitcnt lgkmcnt(10)\n"
        "v_mfma_f32_32x32x16_bf16 v[128:143], v[84:87], v[52:55], v[128:143]\n"
        "ds_read_b128 v[96:99], v242 offset:256\n"
        "ds_read_b64_tr_b16 v[68:69], v241 offset:18432\n"
        "ds_read_b64_tr_b16 v[70:71], v241 offset:18944\n"
        "s_waitcnt lgkmcnt(12)\n"
        "v_mfma_f32_16x16x32_bf16 v[144:147], v[104:107], v[104:107], v[144:147]\n"
        "ds_read_b128 v[100:103], v242 offset:288\n"
        "v_mfma_f32_16x16x32_bf16 v[148:151], v[156:159], v[104:107], v[148:151]\n"
        "s_waitcnt lgkmcnt(10)\n"
        "v_mfma_f32_32x32x16_bf16 v[128:143], v[88:91], v[56:59], v[128:143]\n"
        "ds_read_b64_tr_b16 v[40:41], v241 offset:20480\n"
        "ds_read_b64_tr_b16 v[42:43], v241 offset:20992\n"
        "ds_read_b128 v[72:75], v242 offset:320\n"
        "s_waitcnt lgkmcnt(12)\n"
        "v_mfma_f32_16x16x32_bf16 v[144:147], v[108:111], v[108:111], v[144:147]\n"
        "ds_read_b64_tr_b16 v[44:45], v241 offset:22528\n"
        "v_mfma_f32_16x16x32_bf16 v[148:151], v[156:159], v[108:111], v[148:151]\n"
        "s_waitcnt lgkmcnt(10)\n"
        "v_mfma_f32_32x32x16_bf16 v[128:143], v[92:95], v[60:63], v[128:143]\n"
        "ds_read_b64_tr_b16 v[46:47], v241 offset:23040\n"
        "ds_read_b128 v[76:79], v242 offset:352\n"
        "ds_read_b64_tr_b16 v[48:49], v241 offset:24576\n"
        "s_waitcnt lgkmcnt(10)\n"
        "v_mfma_f32_32x32x16_bf16 v[128:143], v[96:99], v[64:67], v[128:143]\n"
        "ds_read_b64_tr_b16 v[50:51], v241 offset:25088\n"
        "ds_read_b128 v[80:83], v242 offset:384\n"
        "ds_read_b64_tr_b16 v[52:53], v241 offset:26624\n"
        "s_waitcnt lgkmcnt(10)\n"
        "v_mfma_f32_32x32x16_bf16 v[128:143], v[100:103], v[68:71], v[128:143]\n"
        "ds_read_b64_tr_b16 v[54:55], v241 offset:27136\n"
        "ds_read_b128 v[84:87], v242 offset:416\n"
        "s_nop 3\n"
        "v_cndmask_b32 v180, v144, v145, s[46:47]\n"
        "v_cndmask_b32 v180, v180, v146, s[48:49]\n"
        "v_cndmask_b32 v180, v180, v147, s[50:51]\n"
        "v_mul_f32 v181, 0x3b124925, v148\n"
        "s_waitcnt lgkmcnt(9)\n"
        "v_mfma_f32_32x32x16_bf16 v[128:143], v[72:75], v[40:43], v[128:143]\n"
        "v_mul_f32 v180, 0x3b124925, v180\n"
        "v_fma_f32 v180, -v181, v181, v180\n"
        "v_max_f32 v180, 0, v180\n"
        "v_cvt_pk_bf16_f32 v180, v180, v181\n"
        "v_and_b32 v181, 0xffff0000, v180\n"
        "s_waitcnt lgkmcnt(6)\n"
        "v_mfma_f32_32x32x16_bf16 v[128:143], v[76:79], v[44:47], v[128:143]\n"
        "v_lshlrev_b32 v180, 16, v180\n"
        "v_add_f32 v180, 0x3727c5ac, v180\n"
        "v_cvt_pk_bf16_f32 v180, v180, v180\n"
        "v_lshlrev_b32 v180, 16, v180\n"
        "v_sqrt_f32 v180, v180\n"
        "s_waitcnt lgkmcnt(3)\n"
        "v_mfma_f32_32x32x16_bf16 v[128:143], v[80:83], v[48:51], v[128:143]\n"
        "s_nop 0\n"
        "v_cvt_pk_bf16_f32 v180, v180, v180\n"
        "v_lshlrev_b32 v180, 16, v180\n"
        "v_rcp_f32 v180, v180\n"
        "s_nop 0\n"
        "s_waitcnt lgkmcnt(0)\n"
        "v_mfma_f32_32x32x16_bf16 v[128:143], v[84:87], v[52:55], v[128:143]\n"
        ".Lp1j%=:\n"
        "s_mov_b32 exec_lo, 0x00030003\n"
        "s_mov_b32 exec_hi, 0x00030003\n"
        "ds_write_b128 v239, v[152:155]\n"
        "s_mov_b32 exec_lo, 0x00f0000f\n"
        "s_mov_b32 exec_hi, 0xf0000f00\n"
        "ds_write2_b32 v238, v181, v180 offset1:64\n"
        "s_mov_b64 exec, -1\n"
        "s_waitcnt lgkmcnt(0)\n"
        "s_barrier\n"
        "s_cmp_eq_u32 %[mode], 2\n"
        "s_cbranch_scc1 .Lp1ev%=\n"
        "ds_read2_b32 v[180:181], v244 offset1:64\n"
        "ds_read_b128 v[40:43], v245 offset:0\n"
        "ds_read_b128 v[44:47], v245 offset:32\n"
        "ds_read_b128 v[48:51], v245 offset:64\n"
        "ds_read_b128 v[52:55], v245 offset:96\n"
        "ds_read_b128 v[56:59], v245 offset:256\n"
        "ds_read_b128 v[60:63], v245 offset:288\n"
        "ds_read_b128 v[64:67], v245 offset:320\n"
        "ds_read_b128 v[68:71], v245 offset:352\n"
        "s_cmp_eq_u32 %[mode], 0\n"
        "s_cselect_b32 s40, 0x3e8b97f6, 1.0\n"
        "s_waitcnt lgkmcnt(8)\n"
        "v_mul_f32 v182, s40, v181\n"
        "s_waitcnt lgkmcnt(6)\n"
        "v_pk_fma_f32 v[128:129], v[40:41], v[180:181], v[128:129] op_sel_hi:[1,0,1] neg_lo:[0,1,0] neg_hi:[0,1,0]\n"
        "v_pk_fma_f32 v[130:131], v[42:43], v[180:181], v[130:131] op_sel_hi:[1,0,1] neg_lo:[0,1,0] neg_hi:[0,1,0]\n"
        "v_pk_fma_f32 v[132:133], v[44:45], v[180:181], v[132:133] op_sel_hi:[1,0,1] neg_lo:[0,1,0] neg_hi:[0,1,0]\n"
        "v_pk_fma_f32 v[134:135], v[46:47], v[180:181], v[134:135] op_sel_hi:[1,0,1] neg_lo:[0,1,0] neg_hi:[0,1,0]\n"
        "s_waitcnt lgkmcnt(4)\n"
        "v_pk_fma_f32 v[136:137], v[48:49], v[180:181], v[136:137] op_sel_hi:[1,0,1] neg_lo:[0,1,0] neg_hi:[0,1,0]\n"
        "v_pk_fma_f32 v[138:139], v[50:51], v[180:181], v[138:139] op_sel_hi:[1,0,1] neg_lo:[0,1,0] neg_hi:[0,1,0]\n"
        "v_pk_fma_f32 v[140:141], v[52:53], v[180:181], v[140:141] op_sel_hi:[1,0,1] neg_lo:[0,1,0] neg_hi:[0,1,0]\n"
        "v_pk_fma_f32 v[142:143], v[54:55], v[180:181], v[142:143] op_sel_hi:[1,0,1] neg_lo:[0,1,0] neg_hi:[0,1,0]\n"
        "s_waitcnt lgkmcnt(2)\n"
        "v_pk_fma_f32 v[128:129], v[56:57], v[180:181], v[128:129] op_sel_hi:[1,0,1] neg_lo:[0,1,0] neg_hi:[0,1,0]\n"
        "v_pk_fma_f32 v[130:131], v[58:59], v[180:181], v[130:131] op_sel_hi:[1,0,1] neg_lo:[0,1,0] neg_hi:[0,1,0]\n"
        "v_pk_fma_f32 v[132:133], v[60:61], v[180:181], v[132:133] op_sel_hi:[1,0,1] neg_lo:[0,1,0] neg_hi:[0,1,0]\n"
        "v_pk_fma_f32 v[134:135], v[62:63], v[180:181], v[134:135] op_sel_hi:[1,0,1] neg_lo:[0,1,0] neg_hi:[0,1,0]\n"
        "s_waitcnt lgkmcnt(0)\n"
        "v_pk_fma_f32 v[136:137], v[64:65], v[180:181], v[136:137] op_sel_hi:[1,0,1] neg_lo:[0,1,0] neg_hi:[0,1,0]\n"
        "v_pk_fma_f32 v[138:139], v[66:67], v[180:181], v[138:139] op_sel_hi:[1,0,1] neg_lo:[0,1,0] neg_hi:[0,1,0]\n"
        "v_pk_fma_f32 v[140:141], v[68:69], v[180:181], v[140:141] op_sel_hi:[1,0,1] neg_lo:[0,1,0] neg_hi:[0,1,0]\n"
        "v_pk_fma_f32 v[142:143], v[70:71], v[180:181], v[142:143] op_sel_hi:[1,0,1] neg_lo:[0,1,0] neg_hi:[0,1,0]\n"
        "v_pk_mul_f32 v[128:129], v[128:129], v[182:183] op_sel_hi:[1,0]\n"
        "v_pk_mul_f32 v[130:131], v[130:131], v[182:183] op_sel_hi:[1,0]\n"
        "v_pk_mul_f32 v[132:133], v[132:133], v[182:183] op_sel_hi:[1,0]\n"
        "v_pk_mul_f32 v[134:135], v[134:135], v[182:183] op_sel_hi:[1,0]\n"
        "v_pk_mul_f32 v[136:137], v[136:137], v[182:183] op_sel_hi:[1,0]\n"
        "v_pk_mul_f32 v[138:139], v[138:139], v[182:183] op_sel_hi:[1,0]\n"
        "v_pk_mul_f32 v[140:141], v[140:141], v[182:183] op_sel_hi:[1,0]\n"
        "v_pk_mul_f32 v[142:143], v[142:143], v[182:183] op_sel_hi:[1,0]\n"
        "v_cvt_pk_bf16_f32 v72, v128, v129\n"
        "v_cvt_pk_bf16_f32 v73, v130, v131\n"
        "v_cvt_pk_bf16_f32 v74, v132, v133\n"
        "v_cvt_pk_bf16_f32 v75, v134, v135\n"
        "v_cvt_pk_bf16_f32 v76, v136, v137\n"
        "v_cvt_pk_bf16_f32 v77, v138, v139\n"
        "v_cvt_pk_bf16_f32 v78, v140, v141\n"
        "v_cvt_pk_bf16_f32 v79, v142, v143\n"
        "s_cbranch_scc0 .Lp1ek%=\n"
        "buffer_store_dwordx2 v[72:73], v240, %[rs], 0 offen offset:0 sc1\n"
        "buffer_store_dwordx2 v[74:75], v240, %[rs], 0 offen offset:128 sc1\n"
        "buffer_store_dwordx2 v[76:77], v240, %[rs], 0 offen offset:256 sc1\n"
        "buffer_store_dwordx2 v[78:79], v240, %[rs], 0 offen offset:384 sc1\n"
        "s_branch .Lp1d%=\n"
        ".Lp1ek%=:\n"
        "s_mov_b64 exec, s[44:45]\n"
        "v_mov_b32 v78, 0x3f80\n"
        "v_mov_b32 v79, 0\n"
        "s_branch .Lp1s%=\n"
        ".Lp1ev%=:\n"
        "ds_read2_b32 v[180:181], v244 offset1:64\n"
        "ds_read_b128 v[40:43], v245 offset:0\n"
        "ds_read_b128 v[44:47], v245 offset:32\n"
        "ds_read_b128 v[48:51], v245 offset:64\n"
        "ds_read_b128 v[52:55], v245 offset:96\n"
        "ds_read_b128 v[56:59], v245 offset:256\n"
        "ds_read_b128 v[60:63], v245 offset:288\n"
        "ds_read_b128 v[64:67], v245 offset:320\n"
        "ds_read_b128 v[68:71], v245 offset:352\n"
        "s_waitcnt lgkmcnt(4)\n"
        "v_add_f32 v180, v180, v181\n"
        "v_pk_fma_f32 v[128:129], v[40:41], v[180:181], v[128:129] op_sel_hi:[1,0,1] neg_lo:[0,1,0] neg_hi:[0,1,0]\n"
        "v_pk_fma_f32 v[130:131], v[42:43], v[180:181], v[130:131] op_sel_hi:[1,0,1] neg_lo:[0,1,0] neg_hi:[0,1,0]\n"
        "v_pk_fma_f32 v[132:133], v[44:45], v[180:181], v[132:133] op_sel_hi:[1,0,1] neg_lo:[0,1,0] neg_hi:[0,1,0]\n"
        "v_pk_fma_f32 v[134:135], v[46:47], v[180:181], v[134:135] op_sel_hi:[1,0,1] neg_lo:[0,1,0] neg_hi:[0,1,0]\n"
        "v_pk_fma_f32 v[136:137], v[48:49], v[180:181], v[136:137] op_sel_hi:[1,0,1] neg_lo:[0,1,0] neg_hi:[0,1,0]\n"
        "v_pk_fma_f32 v[138:139], v[50:51], v[180:181], v[138:139] op_sel_hi:[1,0,1] neg_lo:[0,1,0] neg_hi:[0,1,0]\n"
        "v_pk_fma_f32 v[140:141], v[52:53], v[180:181], v[140:141] op_sel_hi:[1,0,1] neg_lo:[0,1,0] neg_hi:[0,1,0]\n"
        "v_pk_fma_f32 v[142:143], v[54:55], v[180:181], v[142:143] op_sel_hi:[1,0,1] neg_lo:[0,1,0] neg_hi:[0,1,0]\n"
        "s_waitcnt lgkmcnt(0)\n"
        "v_pk_mul_f32 v[128:129], v[128:129], v[56:57]\n"
        "v_pk_mul_f32 v[130:131], v[130:131], v[58:59]\n"
        "v_pk_mul_f32 v[132:133], v[132:133], v[60:61]\n"
        "v_pk_mul_f32 v[134:135], v[134:135], v[62:63]\n"
        "v_pk_mul_f32 v[136:137], v[136:137], v[64:65]\n"
        "v_pk_mul_f32 v[138:139], v[138:139], v[66:67]\n"
        "v_pk_mul_f32 v[140:141], v[140:141], v[68:69]\n"
        "v_pk_mul_f32 v[142:143], v[142:143], v[70:71]\n"
        "v_cvt_pk_bf16_f32 v72, v128, v129\n"
        "v_cvt_pk_bf16_f32 v73, v130, v131\n"
        "v_cvt_pk_bf16_f32 v74, v132, v133\n"
        "v_cvt_pk_bf16_f32 v75, v134, v135\n"
        "v_cvt_pk_bf16_f32 v76, v136, v137\n"
        "v_cvt_pk_bf16_f32 v77, v138, v139\n"
        "v_cvt_pk_bf16_f32 v78, v140, v141\n"
        "v_cvt_pk_bf16_f32 v79, v142, v143\n"
        "s_mov_b32 exec_lo, 0xf0000000\n"
        "s_mov_b32 exec_hi, 0xf0000000\n"
        "v_mov_b32 v72, v247\n"
        "v_mov_b32 v73, v247\n"
        "v_mov_b32 v74, v247\n"
        "v_mov_b32 v75, v247\n"
        "v_mov_b32 v76, v247\n"
        "v_mov_b32 v77, v247\n"
        "v_mov_b32 v78, v247\n"
        "v_mov_b32 v79, v247\n"
        ".Lp1s%=:\n"
        "s_mov_b64 exec, -1\n"
        "buffer_store_dwordx4 v[72:75], v240, %[rs], 0 offen sc1\n"
        "buffer_store_dwordx4 v[76:79], v240, %[rs], 0 offen offset:1024 sc1\n"
        ".Lp1d%=:\n"
        PROF_STAMP_ASM(2)
        "s_lshr_b32 s55, %[sq], 6\n"
        "s_add_u32 s55, s55, 2797568\n"
        "s_waitcnt vmcnt(0)\n"
        "v_readfirstlane_b32 %[tag], v249\n"
        "s_barrier\n"
        "s_add_u32 %[tag], %[tag], 1\n"
        "s_not_b32 s54, %[tag]\n"
        "v_mov_b32 v180, %[tag]\n"
        "v_mov_b32 v181, s54\n"
        "s_cmp_eq_u32 %[sw], 0\n"
        "s_cselect_b32 exec_lo, 1, 0\n"
        "s_mov_b32 exec_hi, 0\n"
        "buffer_store_dwordx2 v[180:181], off, %[rs], s55 sc1\n"
        "s_mov_b64 exec, -1\n"
        "s_nop 1\n"
        PROF_STAMP_ASM(3)
        : "+{v[232:247]}"(p1), [tag] "=&s"(tag), "+v"(cw) PROF_ASM_OUT1
        : [rs] "s"(rs), [sq] "s"(WS_QF + wg * 8192), [sw] "s"(w), [mode] "s"(__builtin_amdgcn_readfirstlane(mode)),
          [wol] "s"(lb + K2_WOR + w * 7168), [wop] "s"((const char*)wo + 384 * 896 + w * 7168), [xo] "v"(xo),
          [pf] "v"(min(lane >> 2, 6) * 3584 + (lane & 3) * 128),
          [pfb] "s"((const char*)(tt ? wup : wgate) + (14 * (wg >> 3) + 7 * nt) * 3584 + (((wg & 7) * 448) & ~127))
        : "v40", "v41", "v42", "v43", "v44", "v45", "v46", "v47", "v48", "v49", "v50", "v51", "v52", "v53", "v54", "v55", "v56", "v57", "v58", "v59", "v60", "v61", "v62", "v63", "v64", "v65", "v66", "v67", "v68", "v69", "v70", "v71", "v72", "v73", "v74", "v75", "v76", "v77", "v78", "v79", "v80", "v81", "v82", "v83", "v84", "v85", "v86", "v87", "v88", "v89", "v90", "v91", "v92", "v93", "v94", "v95", "v96", "v97", "v98", "v99", "v100", "v101", "v102", "v103", "v104", "v105", "v106", "v107", "v108", "v109", "v110", "v111", "v112", "v113", "v114", "v115", "v116", "v117", "v118", "v119", "v120", "v121", "v122", "v123", "v124", "v125", "v126", "v127", "v128", "v129", "v130", "v131", "v132", "v133", "v134", "v135", "v136", "v137", "v138", "v139", "v140", "v141", "v142", "v143", "v144", "v145", "v146", "v147", "v148", "v149", "v150", "v151", "v152", "v153", "v154", "v155", "v156", "v157", "v158", "v159", "v160", "v161", "v162", "v163", "v164", "v165", "v166", "v167", "v168", "v169", "v170", "v171", "v172", "v173", "v174", "v175", "v176", "v177", "v178", "v179", "v180", "v181", "v182", "v183", "v184", "v185", "v186", "v187", "v188", "v189", "v190", "v191", "v192", "v193", "v194", "v195", "v196", "v197", "v198", "v199", "v200", "v201", "v202", "v203", "v204", "v205", "v206", "v207", "v208", "v209", "v210", "v211", "v212", "v213", "v214", "v215", "v216", "v217", "v218", "v219", "v220", "v221", "v222", "v223", "v224", "v225", "v226", "v227", "v228", "v229", "v230", "v231", "v248", "v249", "v250", "v251", "v252", "v253", "v254", "v255", "s40", "s41", "s42", "s43", "s44", "s45", "s46", "s47", "s48", "s49", "s50", "s51", "s52", "s53", "s54", "s55", "s56", "s57", "scc", "vcc", "memory");
    u32x12 p2;
    {
        const int lc = min(l5, 27);
        const char* fs2 = (const char*)wdown + (32 * max(w - 1, 0) + (lane >> 1)) * 896 + (st2 ? cb2 : 0) * 32 + (lane & 1) * 16;
        p2[0] = xo;
        p2[1] = (l5 >> 3) * 448 + hi * 64 + (lane & 7) * 8;
        p2[2] = lb + K2_TP + w * 1024 + xo;
        p2[3] = lb + K2_O + (lane & 7) * 928 + ((4 * ((w + 3) & 3) + (l5 >> 3)) * 7 + hi) * 4;
        p2[4] = lb + K2_O + (2 * (c & 7) + (c >> 3)) * 464 + g * 16;
        p2[5] = lb + K2_A + (4 * blk + 2 * hi) * 1808 + (112 * w + c) * 4;
        p2[6] = (lane & 8) ? 0xEEEEu : 0x4444u;
        p2[7] = lc * 16;
        p2[8] = (unsigned)(uintptr_t)fs2; p2[9] = (unsigned)((uintptr_t)fs2 >> 32);
        p2[10] = lb + P1_X + (8 * cg + 4 * blk + q4) * 912 + 224 * w + p4 * 8;
        p2[11] = WS_FLAG + (w * 64 + lane) * 128;
        const int tok = 2 * w + hi, token = t0 + tok, col = cb2 * 16 + c, ch = col >> 3, hf = ch >= 28;
        char* tpp = smem + K2_TP + w * 1024 + xo;
        *(u32x4*)tpp = (u32x4){lb + K2_A + tok * 1808 + lc * 32, WS_HROW + token * 1792 + l5 * 16, (unsigned)(token * 896 + l5 * 16), lb + K2_R + tok * 4};
        const unsigned wfo = (8 * g + q4) * 32 + p4 * 8;
        *(u32x4*)(tpp + 4096) = (u32x4){(unsigned)(2047 - token), 0u,
                                        (unsigned)(st1 ? lc * 16 : (2 * hf + ((col >> 2) & 1)) * 448 + (ch - 28 * hf) * 16 + (col & 2) * 4), lb + K2_AL + hi * 912 + lc * 16};
        *(u32x4*)(tpp + 8192) = (u32x4){lb + K2_AL + g * 16 + (st1 ? min(c, 2) * 912 : w * 2048 + (c & 1) * 1024), lb + (st1 ? K2_FW1 : K2_FW + w * 14336) + wfo,
                                        WS_ACT + (st1 ? cb1 * 128 + lane * 8 : w * 7168 + (lane >> 2) * 128 + p4 * 16), lb + K2_AL + (st1 ? 1824 + xo : w * 2048 + lane * 4)};
        *(u32x2*)(tpp + 12288) = (u32x2){(unsigned)(col * 2), lb + K2_R + 32 + ((lane - 2) & 7) * 4};
    }
    const char* fs1 = (const char*)((lane & 1) ? wup : wgate) + (112 * w + min(lane >> 1, 27)) * 3584 + (st1 ? cb1 : 0) * 16;
    float swr;
    u32x8 wrv, cv;
    u32x16 tp, ht;
    unsigned kmax;
    asm volatile(
        "buffer_load_dwordx2 v[182:183], v15, %[rs], 0 offen sc1\n"
        "global_load_dwordx4 v[16:19], v11, %[wrp]\n"
        "global_load_dwordx4 v[20:23], v11, %[wrp] offset:448\n"
        "ds_read_b64_tr_b16 v[24:25], v14 offset:0\n"
        "ds_read_b64_tr_b16 v[26:27], v14 offset:32\n"
        "ds_read_b64_tr_b16 v[28:29], v14 offset:64\n"
        "ds_read_b64_tr_b16 v[30:31], v14 offset:96\n"
        "ds_read_b64_tr_b16 v[32:33], v14 offset:128\n"
        "ds_read_b64_tr_b16 v[34:35], v14 offset:160\n"
        "ds_read_b64_tr_b16 v[36:37], v14 offset:192\n"
        "s_mov_b32 s44, 0\n"
        "s_mov_b32 s45, -1\n"
        "s_not_b32 s54, %[tag]\n"
        "v_mov_b32 v180, %[tag]\n"
        "v_mov_b32 v181, s54\n"
        "v_accvgpr_write_b32 a16, 0\n"
        "v_accvgpr_write_b32 a17, 0\n"
        "v_accvgpr_write_b32 a18, 0\n"
        "v_accvgpr_write_b32 a19, 0\n"
        "v_accvgpr_write_b32 a20, 0\n"
        "v_accvgpr_write_b32 a21, 0\n"
        "v_accvgpr_write_b32 a22, 0\n"
        "v_accvgpr_write_b32 a23, 0\n"
        "v_accvgpr_write_b32 a24, 0\n"
        "v_accvgpr_write_b32 a25, 0\n"
        "v_accvgpr_write_b32 a26, 0\n"
        "v_accvgpr_write_b32 a27, 0\n"
        "v_accvgpr_write_b32 a28, 0\n"
        "v_accvgpr_write_b32 a29, 0\n"
        "v_accvgpr_write_b32 a30, 0\n"
        "v_accvgpr_write_b32 a31, 0\n"
        "v_accvgpr_write_b32 a32, 0\n"
        "v_accvgpr_write_b32 a33, 0\n"
        "v_accvgpr_write_b32 a34, 0\n"
        "v_accvgpr_write_b32 a35, 0\n"
        "v_accvgpr_write_b32 a36, 0\n"
        "v_accvgpr_write_b32 a37, 0\n"
        "v_accvgpr_write_b32 a38, 0\n"
        "v_accvgpr_write_b32 a39, 0\n"
        "v_accvgpr_write_b32 a40, 0\n"
        "v_accvgpr_write_b32 a41, 0\n"
        "v_accvgpr_write_b32 a42, 0\n"
        "v_accvgpr_write_b32 a43, 0\n"
        "v_accvgpr_write_b32 a44, 0\n"
        "v_accvgpr_write_b32 a45, 0\n"
        "v_accvgpr_write_b32 a46, 0\n"
        "v_accvgpr_write_b32 a47, 0\n"
        "v_accvgpr_write_b32 a48, 0\n"
        "v_accvgpr_write_b32 a49, 0\n"
        "v_accvgpr_write_b32 a50, 0\n"
        "v_accvgpr_write_b32 a51, 0\n"
        "v_accvgpr_write_b32 a52, 0\n"
        "v_accvgpr_write_b32 a53, 0\n"
        "v_accvgpr_write_b32 a54, 0\n"
        "v_accvgpr_write_b32 a55, 0\n"
        "v_accvgpr_write_b32 a56, 0\n"
        "v_accvgpr_write_b32 a57, 0\n"
        "v_accvgpr_write_b32 a58, 0\n"
        "v_accvgpr_write_b32 a59, 0\n"
        "v_accvgpr_write_b32 a60, 0\n"
        "v_accvgpr_write_b32 a61, 0\n"
        "v_accvgpr_write_b32 a62, 0\n"
        "v_accvgpr_write_b32 a63, 0\n"
        "v_mov_b32 v40, 0\n"
        "v_mov_b32 v41, 0\n"
        "v_mov_b32 v42, 0\n"
        "v_mov_b32 v43, 0\n"
        "v_mov_b32 v44, 0\n"
        "v_mov_b32 v45, 0\n"
        "v_mov_b32 v46, 0\n"
        "v_mov_b32 v47, 0\n"
        "v_mov_b32 v48, 0\n"
        "v_mov_b32 v49, 0\n"
        "v_mov_b32 v50, 0\n"
        "v_mov_b32 v51, 0\n"
        "v_mov_b32 v52, 0\n"
        "v_mov_b32 v53, 0\n"
        "v_mov_b32 v54, 0\n"
        "v_mov_b32 v55, 0\n"
        "s_lshl_b32 s43, %[sw], 16\n"
        "s_add_u32 s43, s43, 2097152\n"
        "s_lshr_b32 s40, %[sq], 2\n"
        "s_and_b32 s40, s40, 0xc000\n"
        "s_add_u32 s56, s40, s43\n"
        "s_add_u32 s40, s40, 0x4000\n"
        "s_and_b32 s40, s40, 0xc000\n"
        "s_add_u32 s57, s40, s43\n"
        "s_add_u32 s40, s40, 0x4000\n"
        "s_and_b32 s40, s40, 0xc000\n"
        "s_add_u32 s58, s40, s43\n"
        "s_add_u32 s40, s40, 0x4000\n"
        "s_and_b32 s40, s40, 0xc000\n"
        "s_add_u32 s59, s40, s43\n"
        "s_add_u32 s40, s40, 0x4000\n"
        "s_mul_i32 s50, %[sw], 1792\n"
        "s_add_u32 s50, s50, %[sq]\n"
        "s_mul_i32 s42, %[sw], 94208\n"
        "s_add_u32 s42, s42, 2359296\n"
        "s_add_u32 s46, %[sw], 0\n"
        "s_and_b32 s46, s46, 3\n"
        "s_mul_i32 s46, s46, 12288\n"
        "s_add_u32 s46, s46, %[lb]\n"
        "s_add_u32 s47, %[sw], 1\n"
        "s_and_b32 s47, s47, 3\n"
        "s_mul_i32 s47, s47, 12288\n"
        "s_add_u32 s47, s47, %[lb]\n"
        "s_add_u32 s48, %[sw], 2\n"
        "s_and_b32 s48, s48, 3\n"
        "s_mul_i32 s48, s48, 12288\n"
        "s_add_u32 s48, s48, %[lb]\n"
        "s_add_u32 s49, %[sw], 3\n"
        "s_and_b32 s49, s49, 3\n"
        "s_mul_i32 s49, s49, 12288\n"
        "s_add_u32 s49, s49, %[lb]\n"
        "v_add_u32 v226, s46, v4\n"
        "v_add_u32 v227, s47, v4\n"
        "v_add_u32 v228, s48, v4\n"
        "v_add_u32 v229, s49, v4\n"
        "v_mov_b32 v224, 0xdddd\n"
        "s_waitcnt lgkmcnt(0)\n"
        "s_mov_b64 exec, s[44:45]\n"
        "v_mov_b32 v24, 0\n"
        "v_mov_b32 v25, 0\n"
        "v_mov_b32 v26, 0\n"
        "v_mov_b32 v27, 0\n"
        "v_mov_b32 v28, 0\n"
        "v_mov_b32 v29, 0\n"
        "v_mov_b32 v30, 0\n"
        "v_mov_b32 v31, 0\n"
        "v_mov_b32 v32, 0\n"
        "v_mov_b32 v33, 0\n"
        "v_mov_b32 v34, 0\n"
        "v_mov_b32 v35, 0\n"
        "v_mov_b32 v36, 0\n"
        "v_mov_b32 v37, 0\n"
        "s_mov_b64 exec, -1\n"
        "v_mov_b32 v184, 0\n"
        "v_mov_b32 v185, 0\n"
        "s_waitcnt vmcnt(0)\n"
        "s_mov_b32 exec_lo, 0x0fffffff\n"
        "s_mov_b32 exec_hi, 0\n"
        "v_dot2c_f32_bf16 v184, 0x3f803f80, v16\n"
        "v_dot2c_f32_bf16 v185, 0x3f803f80, v17\n"
        "v_dot2c_f32_bf16 v184, 0x3f803f80, v18\n"
        "v_dot2c_f32_bf16 v185, 0x3f803f80, v19\n"
        "v_dot2c_f32_bf16 v184, 0x3f803f80, v20\n"
        "v_dot2c_f32_bf16 v185, 0x3f803f80, v21\n"
        "v_dot2c_f32_bf16 v184, 0x3f803f80, v22\n"
        "v_dot2c_f32_bf16 v185, 0x3f803f80, v23\n"
        "s_mov_b64 exec, -1\n"
        "s_nop 2\n"
        "v_add_f32 v184, v184, v185\n"
        "s_nop 1\n"
        "v_add_f32_dpp v184, v184, v184 quad_perm:[1,0,3,2] row_mask:0xf bank_mask:0xf\n"
        "s_nop 1\n"
        "v_add_f32_dpp v184, v184, v184 quad_perm:[2,3,0,1] row_mask:0xf bank_mask:0xf\n"
        "s_nop 1\n"
        "v_add_f32_dpp v184, v184, v184 row_half_mirror row_mask:0xf bank_mask:0xf\n"
        "s_nop 1\n"
        "v_add_f32_dpp v184, v184, v184 row_mirror row_mask:0xf bank_mask:0xf\n"
        "s_nop 1\n"
        "v_readlane_b32 s54, v184, 0\n"
        "v_readlane_b32 s55, v184, 16\n"
        "s_nop 1\n"
        "v_mov_b32 v184, s54\n"
        "v_add_f32 v184, s55, v184\n"
        "s_nop 0\n"
        "v_readfirstlane_b32 %[swr], v184\n"
        "s_mov_b32 s55, 0x10000\n"
        "s_branch .Lpchk%=\n"
        ".Lpoll%=:\n"
        "buffer_load_dwordx2 v[182:183], v15, %[rs], 0 offen sc1\n"
        ".Lpchk%=:\n"
        "s_sub_u32 s55, s55, 1\n"
        "s_waitcnt vmcnt(0)\n"
        "v_cmp_ne_u64 vcc, v[182:183], v[180:181]\n"
        "s_cmp_eq_u32 s55, 0\n"
        "s_cbranch_scc1 .Lpdone%=\n"
        "s_and_b64 exec, exec, vcc\n"
        "s_cbranch_scc1 .Lpoll%=\n"
        ".Lpdone%=:\n"
        "s_mov_b64 exec, -1\n"
        "s_barrier\n"
        PROF_STAMP_ASM(4)
        "v_add_u32 v14, 0x1000, v4\n"
        "v_add_u32 v15, 0x2000, v4\n"
        "v_add_u32 v11, 0x3000, v4\n"
        "buffer_load_dwordx4 a[64:67], v4, %[rs], s56 offen offset:0 sc1\n"
        "buffer_load_dwordx2 v[192:193], v5, %[rs], s50 offen offset:0 sc1\n"
        "buffer_load_dwordx2 v[194:195], v5, %[rs], s50 offen offset:128 sc1\n"
        "buffer_load_dwordx2 v[196:197], v5, %[rs], s50 offen offset:256 sc1\n"
        "buffer_load_dwordx2 v[198:199], v5, %[rs], s50 offen offset:384 sc1\n"
        "buffer_load_dwordx4 a[68:71], v4, %[rs], s56 offen offset:1024 sc1\n"
        "buffer_load_dwordx4 a[72:75], v4, %[rs], s56 offen offset:2048 sc1\n"
        "buffer_load_dwordx4 a[76:79], v4, %[rs], s56 offen offset:3072 sc1\n"
        "buffer_load_dwordx4 a[80:83], v14, %[rs], s56 offen offset:0 sc1\n"
        "buffer_load_dwordx4 a[84:87], v14, %[rs], s56 offen offset:1024 sc1\n"
        "buffer_load_dwordx4 a[88:91], v14, %[rs], s56 offen offset:2048 sc1\n"
        "buffer_load_dwordx4 a[92:95], v14, %[rs], s56 offen offset:3072 sc1\n"
        "s_waitcnt vmcnt(9)\n"
        "v_mfma_f32_32x32x16_bf16 v[128:143], a[64:67], v[192:195], 0\n"
        "ds_write_b128 v226, v[192:195]\n"
        "s_waitcnt vmcnt(7)\n"
        "v_cndmask_b32 v198, v198, 0, s[44:45]\n"
        "v_cndmask_b32 v199, v199, 0, s[44:45]\n"
        "s_waitcnt vmcnt(6)\n"
        "s_nop 1\n"
        "v_mfma_f32_32x32x16_bf16 v[144:159], a[68:71], v[196:199], v[128:143]\n"
        "s_nop 7\n"
        "s_nop 3\n"
        "v_max3_f32 v225, v144, v149, v154\n"
        "v_max3_f32 v225, v225, v147, v159\n"
        "v_cvt_pk_bf16_f32 v225, -v225, 0\n"
        "v_cndmask_b32 v198, v198, v225, s[44:45]\n"
        "s_nop 0\n"
        "ds_write_b128 v226, v[196:199] offset:1024\n"
        "v_mfma_f32_32x32x16_bf16 v[128:143], a[68:71], v[196:199], v[128:143]\n"
        "v_add_u32 v5, %[wl2], v4\n"
        "s_mov_b32 m0, %[wl2]\n"
        "s_waitcnt lgkmcnt(0)\n"
        "s_barrier\n"
        "ds_read_b128 v[200:203], v227 offset:0\n"
        "ds_read_b128 v[204:207], v227 offset:1024\n"
        "ds_read_b128 v[208:211], v228 offset:0\n"
        "ds_read_b128 v[212:215], v228 offset:1024\n"
        "ds_read_b128 v[216:219], v229 offset:0\n"
        "ds_read_b128 v[220:223], v229 offset:1024\n"
        "s_nop 3\n"
        PROF_STAMP_ASM(5)
        "s_waitcnt lgkmcnt(4)\n"
        "v_mfma_f32_32x32x16_bf16 v[144:159], a[64:67], v[200:203], 0\n"
        "buffer_load_dwordx4 a[96:99], v15, %[rs], s56 offen offset:0 sc1\n"
        "v_exp_f32 v128, v128\n"
        "v_exp_f32 v129, v129\n"
        "v_exp_f32 v130, v130\n"
        "v_exp_f32 v131, v131\n"
        "v_mfma_f32_32x32x16_bf16 v[144:159], a[68:71], v[204:207], v[144:159]\n"
        "buffer_load_dwordx4 a[100:103], v15, %[rs], s56 offen offset:1024 sc1\n"
        "v_exp_f32 v132, v132\n"
        "v_exp_f32 v133, v133\n"
        "v_exp_f32 v134, v134\n"
        "v_exp_f32 v135, v135\n"
        "s_waitcnt vmcnt(7)\n"
        "v_exp_f32 v136, v136\n"
        "v_exp_f32 v137, v137\n"
        "v_exp_f32 v138, v138\n"
        "v_exp_f32 v139, v139\n"
        "v_smfmac_f32_32x32x32_bf16 a[16:31], a[72:75], v[128:135], v224\n"
        "buffer_load_dwordx4 a[104:107], v15, %[rs], s56 offen offset:2048 sc1\n"
        "v_exp_f32 v140, v140\n"
        "v_exp_f32 v141, v141\n"
        "v_exp_f32 v142, v142\n"
        "v_exp_f32 v143, v143\n"
        "s_waitcnt lgkmcnt(2)\n"
        "v_mfma_f32_32x32x16_bf16 v[160:175], a[64:67], v[208:211], 0\n"
        "buffer_load_dwordx4 a[108:111], v15, %[rs], s56 offen offset:3072 sc1\n"
        "v_exp_f32 v144, v144\n"
        "v_exp_f32 v145, v145\n"
        "v_exp_f32 v146, v146\n"
        "v_exp_f32 v147, v147\n"
        "v_mfma_f32_32x32x16_bf16 v[160:175], a[68:71], v[212:215], v[160:175]\n"
        "s_waitcnt vmcnt(8)\n"
        "v_exp_f32 v148, v148\n"
        "v_exp_f32 v149, v149\n"
        "v_exp_f32 v150, v150\n"
        "v_exp_f32 v151, v151\n"
        "v_smfmac_f32_32x32x32_bf16 a[16:31], a[76:79], v[136:143], v224\n"
        "buffer_load_dwordx4 a[112:115], v11, %[rs], s56 offen offset:0 sc1\n"
        "v_exp_f32 v152, v152\n"
        "v_exp_f32 v153, v153\n"
        "v_exp_f32 v154, v154\n"
        "v_exp_f32 v155, v155\n"
        "v_smfmac_f32_32x32x32_bf16 a[32:47], a[72:75], v[144:151], v224\n"
        "buffer_load_dwordx4 a[116:119], v11, %[rs], s56 offen offset:1024 sc1\n"
        "v_exp_f32 v156, v156\n"
        "v_exp_f32 v157, v157\n"
        "v_exp_f32 v158, v158\n"
        "v_exp_f32 v159, v159\n"
        "s_waitcnt lgkmcnt(0)\n"
        "v_mfma_f32_32x32x16_bf16 v[176:191], a[64:67], v[216:219], 0\n"
        "buffer_load_dwordx4 a[120:123], v11, %[rs], s56 offen offset:2048 sc1\n"
        "v_exp_f32 v160, v160\n"
        "v_exp_f32 v161, v161\n"
        "v_exp_f32 v162, v162\n"
        "v_exp_f32 v163, v163\n"
        "v_mfma_f32_32x32x16_bf16 v[176:191], a[68:71], v[220:223], v[176:191]\n"
        "buffer_load_dwordx4 a[124:127], v11, %[rs], s56 offen offset:3072 sc1\n"
        "v_exp_f32 v164, v164\n"
        "v_exp_f32 v165, v165\n"
        "v_exp_f32 v166, v166\n"
        "v_exp_f32 v167, v167\n"
        "v_smfmac_f32_32x32x32_bf16 a[32:47], a[76:79], v[152:159], v224\n"
        "buffer_load_dwordx4 a[64:67], v4, %[rs], s57 offen offset:0 sc1\n"
        "v_exp_f32 v168, v168\n"
        "v_exp_f32 v169, v169\n"
        "v_exp_f32 v170, v170\n"
        "v_exp_f32 v171, v171\n"
        "v_smfmac_f32_32x32x32_bf16 a[48:63], a[72:75], v[160:167], v224\n"
        "s_waitcnt vmcnt(12)\n"
        "v_exp_f32 v172, v172\n"
        "v_exp_f32 v173, v173\n"
        "v_exp_f32 v174, v174\n"
        "v_exp_f32 v175, v175\n"
        "v_mfma_f32_32x32x16_bf16 v[128:143], a[80:83], v[192:195], 0\n"
        "s_waitcnt vmcnt(11)\n"
        "v_exp_f32 v176, v176\n"
        "v_exp_f32 v177, v177\n"
        "v_exp_f32 v178, v178\n"
        "v_exp_f32 v179, v179\n"
        "v_mfma_f32_32x32x16_bf16 v[128:143], a[84:87], v[196:199], v[128:143]\n"
        "buffer_load_dwordx4 a[68:71], v4, %[rs], s57 offen offset:1024 sc1\n"
        "v_exp_f32 v180, v180\n"
        "v_exp_f32 v181, v181\n"
        "v_exp_f32 v182, v182\n"
        "v_exp_f32 v183, v183\n"
        "v_smfmac_f32_32x32x32_bf16 a[48:63], a[76:79], v[168:175], v224\n"
        "v_exp_f32 v184, v184\n"
        "v_exp_f32 v185, v185\n"
        "v_exp_f32 v186, v186\n"
        "v_exp_f32 v187, v187\n"
        "v_smfmac_f32_32x32x32_bf16 v[40:55], a[72:75], v[176:183], v224\n"
        "buffer_load_dwordx4 a[72:75], v4, %[rs], s57 offen offset:2048 sc1\n"
        "v_exp_f32 v188, v188\n"
        "v_exp_f32 v189, v189\n"
        "v_exp_f32 v190, v190\n"
        "v_exp_f32 v191, v191\n"
        "v_mfma_f32_32x32x16_bf16 v[144:159], a[80:83], v[200:203], 0\n"
        "v_exp_f32 v128, v128\n"
        "v_exp_f32 v129, v129\n"
        "v_exp_f32 v130, v130\n"
        "v_exp_f32 v131, v131\n"
        "v_mfma_f32_32x32x16_bf16 v[144:159], a[84:87], v[204:207], v[144:159]\n"
        "v_exp_f32 v132, v132\n"
        "v_exp_f32 v133, v133\n"
        "v_exp_f32 v134, v134\n"
        "v_exp_f32 v135, v135\n"
        "v_smfmac_f32_32x32x32_bf16 v[40:55], a[76:79], v[184:191], v224\n"
        "s_waitcnt vmcnt(12)\n"
        "v_exp_f32 v136, v136\n"
        "v_exp_f32 v137, v137\n"
        "v_exp_f32 v138, v138\n"
        "v_exp_f32 v139, v139\n"
        "v_smfmac_f32_32x32x32_bf16 a[16:31], a[88:91], v[128:135], v224\n"
        "buffer_load_dwordx4 a[76:79], v4, %[rs], s57 offen offset:3072 sc1\n"
        "v_exp_f32 v140, v140\n"
        "v_exp_f32 v141, v141\n"
        "v_exp_f32 v142, v142\n"
        "v_exp_f32 v143, v143\n"
        "v_mfma_f32_32x32x16_bf16 v[160:175], a[80:83], v[208:211], 0\n"
        "v_exp_f32 v144, v144\n"
        "v_exp_f32 v145, v145\n"
        "v_exp_f32 v146, v146\n"
        "v_exp_f32 v147, v147\n"
        "v_mfma_f32_32x32x16_bf16 v[160:175], a[84:87], v[212:215], v[160:175]\n"
        "s_waitcnt vmcnt(12)\n"
        "v_exp_f32 v148, v148\n"
        "v_exp_f32 v149, v149\n"
        "v_exp_f32 v150, v150\n"
        "v_exp_f32 v151, v151\n"
        "v_smfmac_f32_32x32x32_bf16 a[16:31], a[92:95], v[136:143], v224\n"
        "buffer_load_dwordx4 v4, %[rs], s42 offen offset:0 sc1 lds\n"
        "v_exp_f32 v152, v152\n"
        "v_exp_f32 v153, v153\n"
        "v_exp_f32 v154, v154\n"
        "v_exp_f32 v155, v155\n"
        "v_smfmac_f32_32x32x32_bf16 a[32:47], a[88:91], v[144:151], v224\n"
        "buffer_load_dwordx4 v4, %[rs], s42 offen offset:1024 sc1 lds\n"
        "v_exp_f32 v156, v156\n"
        "v_exp_f32 v157, v157\n"
        "v_exp_f32 v158, v158\n"
        "v_exp_f32 v159, v159\n"
        "v_mfma_f32_32x32x16_bf16 v[176:191], a[80:83], v[216:219], 0\n"
        "buffer_load_dwordx4 a[80:83], v14, %[rs], s57 offen offset:0 sc1\n"
        "v_exp_f32 v160, v160\n"
        "v_exp_f32 v161, v161\n"
        "v_exp_f32 v162, v162\n"
        "v_exp_f32 v163, v163\n"
        "v_mfma_f32_32x32x16_bf16 v[176:191], a[84:87], v[220:223], v[176:191]\n"
        "buffer_load_dwordx4 a[84:87], v14, %[rs], s57 offen offset:1024 sc1\n"
        "v_exp_f32 v164, v164\n"
        "v_exp_f32 v165, v165\n"
        "v_exp_f32 v166, v166\n"
        "v_exp_f32 v167, v167\n"
        "v_smfmac_f32_32x32x32_bf16 a[32:47], a[92:95], v[152:159], v224\n"
        "s_load_dword s53, %[pd], 0x0\n"
        "v_exp_f32 v168, v168\n"
        "v_exp_f32 v169, v169\n"
        "v_exp_f32 v170, v170\n"
        "v_exp_f32 v171, v171\n"
        "v_smfmac_f32_32x32x32_bf16 a[48:63], a[88:91], v[160:167], v224\n"
        "s_waitcnt vmcnt(15)\n"
        "v_exp_f32 v172, v172\n"
        "v_exp_f32 v173, v173\n"
        "v_exp_f32 v174, v174\n"
        "v_exp_f32 v175, v175\n"
        "v_mfma_f32_32x32x16_bf16 v[128:143], a[96:99], v[192:195], 0\n"
        "s_waitcnt vmcnt(14)\n"
        "v_exp_f32 v176, v176\n"
        "v_exp_f32 v177, v177\n"
        "v_exp_f32 v178, v178\n"
        "v_exp_f32 v179, v179\n"
        "v_mfma_f32_32x32x16_bf16 v[128:143], a[100:103], v[196:199], v[128:143]\n"
        "buffer_load_dwordx4 v4, %[rs], s42 offen offset:2048 sc1 lds\n"
        "v_exp_f32 v180, v180\n"
        "v_exp_f32 v181, v181\n"
        "v_exp_f32 v182, v182\n"
        "v_exp_f32 v183, v183\n"
        "v_smfmac_f32_32x32x32_bf16 a[48:63], a[92:95], v[168:175], v224\n"
        "buffer_load_dwordx4 v4, %[rs], s42 offen offset:3072 sc1 lds\n"
        "v_exp_f32 v184, v184\n"
        "v_exp_f32 v185, v185\n"
        "v_exp_f32 v186, v186\n"
        "v_exp_f32 v187, v187\n"
        "v_smfmac_f32_32x32x32_bf16 v[40:55], a[88:91], v[176:183], v224\n"
        "buffer_load_dwordx4 a[88:91], v14, %[rs], s57 offen offset:2048 sc1\n"
        "v_exp_f32 v188, v188\n"
        "v_exp_f32 v189, v189\n"
        "v_exp_f32 v190, v190\n"
        "v_exp_f32 v191, v191\n"
        "v_mfma_f32_32x32x16_bf16 v[144:159], a[96:99], v[200:203], 0\n"
        "s_load_dword s53, %[pd], 0x40\n"
        "v_exp_f32 v128, v128\n"
        "v_exp_f32 v129, v129\n"
        "v_exp_f32 v130, v130\n"
        "v_exp_f32 v131, v131\n"
        "v_mfma_f32_32x32x16_bf16 v[144:159], a[100:103], v[204:207], v[144:159]\n"
        "v_exp_f32 v132, v132\n"
        "v_exp_f32 v133, v133\n"
        "v_exp_f32 v134, v134\n"
        "v_exp_f32 v135, v135\n"
        "v_smfmac_f32_32x32x32_bf16 v[40:55], a[92:95], v[184:191], v224\n"
        "s_waitcnt vmcnt(15)\n"
        "v_exp_f32 v136, v136\n"
        "v_exp_f32 v137, v137\n"
        "v_exp_f32 v138, v138\n"
        "v_exp_f32 v139, v139\n"
        "v_smfmac_f32_32x32x32_bf16 a[16:31], a[104:107], v[128:135], v224\n"
        "buffer_load_dwordx4 a[92:95], v14, %[rs], s57 offen offset:3072 sc1\n"
        "v_exp_f32 v140, v140\n"
        "v_exp_f32 v141, v141\n"
        "v_exp_f32 v142, v142\n"
        "v_exp_f32 v143, v143\n"
        "v_mfma_f32_32x32x16_bf16 v[160:175], a[96:99], v[208:211], 0\n"
        "s_add_u32 m0, m0, 0x1000\n"
        "v_exp_f32 v144, v144\n"
        "v_exp_f32 v145, v145\n"
        "v_exp_f32 v146, v146\n"
        "v_exp_f32 v147, v147\n"
        "v_mfma_f32_32x32x16_bf16 v[160:175], a[100:103], v[212:215], v[160:175]\n"
        "buffer_load_dwordx4 v14, %[rs], s42 offen offset:0 sc1 lds\n"
        "v_exp_f32 v148, v148\n"
        "v_exp_f32 v149, v149\n"
        "v_exp_f32 v150, v150\n"
        "v_exp_f32 v151, v151\n"
        "v_smfmac_f32_32x32x32_bf16 a[16:31], a[108:111], v[136:143], v224\n"
        "buffer_load_dwordx4 v14, %[rs], s42 offen offset:1024 sc1 lds\n"
        "v_exp_f32 v152, v152\n"
        "v_exp_f32 v153, v153\n"
        "v_exp_f32 v154, v154\n"
        "v_exp_f32 v155, v155\n"
        "v_smfmac_f32_32x32x32_bf16 a[32:47], a[104:107], v[144:151], v224\n"
        "s_load_dword s53, %[pd], 0x380\n"
        "v_exp_f32 v156, v156\n"
        "v_exp_f32 v157, v157\n"
        "v_exp_f32 v158, v158\n"
        "v_exp_f32 v159, v159\n"
        "v_mfma_f32_32x32x16_bf16 v[176:191], a[96:99], v[216:219], 0\n"
        "buffer_load_dwordx4 a[96:99], v15, %[rs], s57 offen offset:0 sc1\n"
        "v_exp_f32 v160, v160\n"
        "v_exp_f32 v161, v161\n"
        "v_exp_f32 v162, v162\n"
        "v_exp_f32 v163, v163\n"
        "v_mfma_f32_32x32x16_bf16 v[176:191], a[100:103], v[220:223], v[176:191]\n"
        "buffer_load_dwordx4 a[100:103], v15, %[rs], s57 offen offset:1024 sc1\n"
        "v_exp_f32 v164, v164\n"
        "v_exp_f32 v165, v165\n"
        "v_exp_f32 v166, v166\n"
        "v_exp_f32 v167, v167\n"
        "v_smfmac_f32_32x32x32_bf16 a[32:47], a[108:111], v[152:159], v224\n"
        "buffer_load_dwordx4 v14, %[rs], s42 offen offset:2048 sc1 lds\n"
        "v_exp_f32 v168, v168\n"
        "v_exp_f32 v169, v169\n"
        "v_exp_f32 v170, v170\n"
        "v_exp_f32 v171, v171\n"
        "v_smfmac_f32_32x32x32_bf16 a[48:63], a[104:107], v[160:167], v224\n"
        "s_waitcnt vmcnt(17)\n"
        "v_exp_f32 v172, v172\n"
        "v_exp_f32 v173, v173\n"
        "v_exp_f32 v174, v174\n"
        "v_exp_f32 v175, v175\n"
        "v_mfma_f32_32x32x16_bf16 v[128:143], a[112:115], v[192:195], 0\n"
        "buffer_load_dwordx4 a[128:131], v15, %[rs], s42 offen offset:0 sc1\n"
        "v_exp_f32 v176, v176\n"
        "v_exp_f32 v177, v177\n"
        "v_exp_f32 v178, v178\n"
        "v_exp_f32 v179, v179\n"
        "v_mfma_f32_32x32x16_bf16 v[128:143], a[116:119], v[196:199], v[128:143]\n"
        "s_mov_b32 m0, %[fl]\n"
        "v_exp_f32 v180, v180\n"
        "v_exp_f32 v181, v181\n"
        "v_exp_f32 v182, v182\n"
        "v_exp_f32 v183, v183\n"
        "v_smfmac_f32_32x32x32_bf16 a[48:63], a[108:111], v[168:175], v224\n"
        "s_load_dword s53, %[pd], 0x3c0\n"
        "v_exp_f32 v184, v184\n"
        "v_exp_f32 v185, v185\n"
        "v_exp_f32 v186, v186\n"
        "v_exp_f32 v187, v187\n"
        "v_smfmac_f32_32x32x32_bf16 v[40:55], a[104:107], v[176:183], v224\n"
        "buffer_load_dwordx4 a[104:107], v15, %[rs], s57 offen offset:2048 sc1\n"
        "v_exp_f32 v188, v188\n"
        "v_exp_f32 v189, v189\n"
        "v_exp_f32 v190, v190\n"
        "v_exp_f32 v191, v191\n"
        "v_mfma_f32_32x32x16_bf16 v[144:159], a[112:115], v[200:203], 0\n"
        "buffer_load_dwordx4 a[132:135], v15, %[rs], s42 offen offset:1024 sc1\n"
        "v_exp_f32 v128, v128\n"
        "v_exp_f32 v129, v129\n"
        "v_exp_f32 v130, v130\n"
        "v_exp_f32 v131, v131\n"
        "v_mfma_f32_32x32x16_bf16 v[144:159], a[116:119], v[204:207], v[144:159]\n"
        "buffer_load_dwordx4 a[136:139], v15, %[rs], s42 offen offset:2048 sc1\n"
        "v_exp_f32 v132, v132\n"
        "v_exp_f32 v133, v133\n"
        "v_exp_f32 v134, v134\n"
        "v_exp_f32 v135, v135\n"
        "v_smfmac_f32_32x32x32_bf16 v[40:55], a[108:111], v[184:191], v224\n"
        "buffer_load_dwordx4 a[108:111], v15, %[rs], s57 offen offset:3072 sc1\n"
        "v_exp_f32 v136, v136\n"
        "v_exp_f32 v137, v137\n"
        "v_exp_f32 v138, v138\n"
        "v_exp_f32 v139, v139\n"
        "v_smfmac_f32_32x32x32_bf16 a[16:31], a[120:123], v[128:135], v224\n"
        "s_mov_b32 s50, 0x18800\n"
        "v_exp_f32 v140, v140\n"
        "v_exp_f32 v141, v141\n"
        "v_exp_f32 v142, v142\n"
        "v_exp_f32 v143, v143\n"
        "v_mfma_f32_32x32x16_bf16 v[160:175], a[112:115], v[208:211], 0\n"
        "s_mov_b32 s51, 0\n"
        "v_exp_f32 v144, v144\n"
        "v_exp_f32 v145, v145\n"
        "v_exp_f32 v146, v146\n"
        "v_exp_f32 v147, v147\n"
        "v_mfma_f32_32x32x16_bf16 v[160:175], a[116:119], v[212:215], v[160:175]\n"
        "s_load_dword s53, %[pd], 0x700\n"
        "v_exp_f32 v148, v148\n"
        "v_exp_f32 v149, v149\n"
        "v_exp_f32 v150, v150\n"
        "v_exp_f32 v151, v151\n"
        "v_smfmac_f32_32x32x32_bf16 a[16:31], a[124:127], v[136:143], v224\n"
        "buffer_load_dwordx4 a[140:143], v15, %[rs], s42 offen offset:3072 sc1\n"
        "v_exp_f32 v152, v152\n"
        "v_exp_f32 v153, v153\n"
        "v_exp_f32 v154, v154\n"
        "v_exp_f32 v155, v155\n"
        "v_smfmac_f32_32x32x32_bf16 a[32:47], a[120:123], v[144:151], v224\n"
        "buffer_load_dwordx4 a[144:147], v11, %[rs], s42 offen offset:0 sc1\n"
        "v_exp_f32 v156, v156\n"
        "v_exp_f32 v157, v157\n"
        "v_exp_f32 v158, v158\n"
        "v_exp_f32 v159, v159\n"
        "v_mfma_f32_32x32x16_bf16 v[176:191], a[112:115], v[216:219], 0\n"
        "buffer_load_dwordx4 a[112:115], v11, %[rs], s57 offen offset:0 sc1\n"
        "v_exp_f32 v160, v160\n"
        "v_exp_f32 v161, v161\n"
        "v_exp_f32 v162, v162\n"
        "v_exp_f32 v163, v163\n"
        "v_mfma_f32_32x32x16_bf16 v[176:191], a[116:119], v[220:223], v[176:191]\n"
        "buffer_load_dwordx4 a[116:119], v11, %[rs], s57 offen offset:1024 sc1\n"
        "v_exp_f32 v164, v164\n"
        "v_exp_f32 v165, v165\n"
        "v_exp_f32 v166, v166\n"
        "v_exp_f32 v167, v167\n"
        "v_smfmac_f32_32x32x32_bf16 a[32:47], a[124:127], v[152:159], v224\n"
        "s_mov_b32 s54, %[gex]\n"
        "v_exp_f32 v168, v168\n"
        "v_exp_f32 v169, v169\n"
        "v_exp_f32 v170, v170\n"
        "v_exp_f32 v171, v171\n"
        "v_smfmac_f32_32x32x32_bf16 a[48:63], a[120:123], v[160:167], v224\n"
        "s_waitcnt vmcnt(22)\n"
        "v_exp_f32 v172, v172\n"
        "v_exp_f32 v173, v173\n"
        "v_exp_f32 v174, v174\n"
        "v_exp_f32 v175, v175\n"
        "v_mfma_f32_32x32x16_bf16 v[128:143], a[64:67], v[192:195], 0\n"
        "s_load_dword s53, %[pd], 0x740\n"
        "v_exp_f32 v176, v176\n"
        "v_exp_f32 v177, v177\n"
        "v_exp_f32 v178, v178\n"
        "v_exp_f32 v179, v179\n"
        "v_mfma_f32_32x32x16_bf16 v[128:143], a[68:71], v[196:199], v[128:143]\n"
        "buffer_load_dwordx4 a[148:151], v11, %[rs], s42 offen offset:1024 sc1\n"
        "v_exp_f32 v180, v180\n"
        "v_exp_f32 v181, v181\n"
        "v_exp_f32 v182, v182\n"
        "v_exp_f32 v183, v183\n"
        "v_smfmac_f32_32x32x32_bf16 a[48:63], a[124:127], v[168:175], v224\n"
        "buffer_load_dwordx4 a[152:155], v11, %[rs], s42 offen offset:2048 sc1\n"
        "v_exp_f32 v184, v184\n"
        "v_exp_f32 v185, v185\n"
        "v_exp_f32 v186, v186\n"
        "v_exp_f32 v187, v187\n"
        "v_smfmac_f32_32x32x32_bf16 v[40:55], a[120:123], v[176:183], v224\n"
        "buffer_load_dwordx4 a[120:123], v11, %[rs], s57 offen offset:2048 sc1\n"
        "v_exp_f32 v188, v188\n"
        "v_exp_f32 v189, v189\n"
        "v_exp_f32 v190, v190\n"
        "v_exp_f32 v191, v191\n"
        "v_mfma_f32_32x32x16_bf16 v[144:159], a[64:67], v[200:203], 0\n"
        "s_lshr_b32 s55, %[gex], 8\n"
        "v_exp_f32 v128, v128\n"
        "v_exp_f32 v129, v129\n"
        "v_exp_f32 v130, v130\n"
        "v_exp_f32 v131, v131\n"
        "v_mfma_f32_32x32x16_bf16 v[144:159], a[68:71], v[204:207], v[144:159]\n"
        "buffer_load_dwordx4 a[156:159], v11, %[rs], s42 offen offset:3072 sc1\n"
        "v_exp_f32 v132, v132\n"
        "v_exp_f32 v133, v133\n"
        "v_exp_f32 v134, v134\n"
        "v_exp_f32 v135, v135\n"
        "v_smfmac_f32_32x32x32_bf16 v[40:55], a[124:127], v[184:191], v224\n"
        "buffer_load_dwordx4 a[124:127], v11, %[rs], s57 offen offset:3072 sc1\n"
        "v_exp_f32 v136, v136\n"
        "v_exp_f32 v137, v137\n"
        "v_exp_f32 v138, v138\n"
        "v_exp_f32 v139, v139\n"
        "v_smfmac_f32_32x32x32_bf16 a[16:31], a[72:75], v[128:135], v224\n"
        "v_exp_f32 v140, v140\n"
        "v_exp_f32 v141, v141\n"
        "v_exp_f32 v142, v142\n"
        "v_exp_f32 v143, v143\n"
        "v_mfma_f32_32x32x16_bf16 v[160:175], a[64:67], v[208:211], 0\n"
        "s_load_dword s53, %[pd], 0xa80\n"
        "v_exp_f32 v144, v144\n"
        "v_exp_f32 v145, v145\n"
        "v_exp_f32 v146, v146\n"
        "v_exp_f32 v147, v147\n"
        "v_mfma_f32_32x32x16_bf16 v[160:175], a[68:71], v[212:215], v[160:175]\n"
        "s_add_u32 s42, s42, 0x4000\n"
        "v_exp_f32 v148, v148\n"
        "v_exp_f32 v149, v149\n"
        "v_exp_f32 v150, v150\n"
        "v_exp_f32 v151, v151\n"
        "v_smfmac_f32_32x32x32_bf16 a[16:31], a[76:79], v[136:143], v224\n"
        "buffer_load_dwordx4 a[160:163], v4, %[rs], s42 offen offset:0 sc1\n"
        "v_exp_f32 v152, v152\n"
        "v_exp_f32 v153, v153\n"
        "v_exp_f32 v154, v154\n"
        "v_exp_f32 v155, v155\n"
        "v_smfmac_f32_32x32x32_bf16 a[32:47], a[72:75], v[144:151], v224\n"
        "buffer_load_dwordx4 a[164:167], v4, %[rs], s42 offen offset:1024 sc1\n"
        "v_exp_f32 v156, v156\n"
        "v_exp_f32 v157, v157\n"
        "v_exp_f32 v158, v158\n"
        "v_exp_f32 v159, v159\n"
        "v_mfma_f32_32x32x16_bf16 v[176:191], a[64:67], v[216:219], 0\n"
        "buffer_load_dwordx4 a[64:67], v4, %[rs], s58 offen offset:0 sc1\n"
        "v_exp_f32 v160, v160\n"
        "v_exp_f32 v161, v161\n"
        "v_exp_f32 v162, v162\n"
        "v_exp_f32 v163, v163\n"
        "v_mfma_f32_32x32x16_bf16 v[176:191], a[68:71], v[220:223], v[176:191]\n"
        "buffer_load_dwordx4 a[68:71], v4, %[rs], s58 offen offset:1024 sc1\n"
        "v_exp_f32 v164, v164\n"
        "v_exp_f32 v165, v165\n"
        "v_exp_f32 v166, v166\n"
        "v_exp_f32 v167, v167\n"
        "v_smfmac_f32_32x32x32_bf16 a[32:47], a[76:79], v[152:159], v224\n"
        "v_exp_f32 v168, v168\n"
        "v_exp_f32 v169, v169\n"
        "v_exp_f32 v170, v170\n"
        "v_exp_f32 v171, v171\n"
        "v_smfmac_f32_32x32x32_bf16 a[48:63], a[72:75], v[160:167], v224\n"
        "s_waitcnt vmcnt(23)\n"
        "v_exp_f32 v172, v172\n"
        "v_exp_f32 v173, v173\n"
        "v_exp_f32 v174, v174\n"
        "v_exp_f32 v175, v175\n"
        "v_mfma_f32_32x32x16_bf16 v[128:143], a[80:83], v[192:195], 0\n"
        "s_load_dword s53, %[pd], 0xac0\n"
        "v_exp_f32 v176, v176\n"
        "v_exp_f32 v177, v177\n"
        "v_exp_f32 v178, v178\n"
        "v_exp_f32 v179, v179\n"
        "v_mfma_f32_32x32x16_bf16 v[128:143], a[84:87], v[196:199], v[128:143]\n"
        "buffer_load_dwordx4 a[168:171], v4, %[rs], s42 offen offset:2048 sc1\n"
        "v_exp_f32 v180, v180\n"
        "v_exp_f32 v181, v181\n"
        "v_exp_f32 v182, v182\n"
        "v_exp_f32 v183, v183\n"
        "v_smfmac_f32_32x32x32_bf16 a[48:63], a[76:79], v[168:175], v224\n"
        "buffer_load_dwordx4 a[172:175], v4, %[rs], s42 offen offset:3072 sc1\n"
        "v_exp_f32 v184, v184\n"
        "v_exp_f32 v185, v185\n"
        "v_exp_f32 v186, v186\n"
        "v_exp_f32 v187, v187\n"
        "v_smfmac_f32_32x32x32_bf16 v[40:55], a[72:75], v[176:183], v224\n"
        "buffer_load_dwordx4 a[72:75], v4, %[rs], s58 offen offset:2048 sc1\n"
        "v_exp_f32 v188, v188\n"
        "v_exp_f32 v189, v189\n"
        "v_exp_f32 v190, v190\n"
        "v_exp_f32 v191, v191\n"
        "v_mfma_f32_32x32x16_bf16 v[144:159], a[80:83], v[200:203], 0\n"
        "v_exp_f32 v128, v128\n"
        "v_exp_f32 v129, v129\n"
        "v_exp_f32 v130, v130\n"
        "v_exp_f32 v131, v131\n"
        "v_mfma_f32_32x32x16_bf16 v[144:159], a[84:87], v[204:207], v[144:159]\n"
        "buffer_load_dwordx4 a[176:179], v14, %[rs], s42 offen offset:0 sc1\n"
        "v_exp_f32 v132, v132\n"
        "v_exp_f32 v133, v133\n"
        "v_exp_f32 v134, v134\n"
        "v_exp_f32 v135, v135\n"
        "v_smfmac_f32_32x32x32_bf16 v[40:55], a[76:79], v[184:191], v224\n"
        "buffer_load_dwordx4 a[76:79], v4, %[rs], s58 offen offset:3072 sc1\n"
        "v_exp_f32 v136, v136\n"
        "v_exp_f32 v137, v137\n"
        "v_exp_f32 v138, v138\n"
        "v_exp_f32 v139, v139\n"
        "v_smfmac_f32_32x32x32_bf16 a[16:31], a[88:91], v[128:135], v224\n"
        "s_load_dword s53, %[pd], 0xe00\n"
        "v_exp_f32 v140, v140\n"
        "v_exp_f32 v141, v141\n"
        "v_exp_f32 v142, v142\n"
        "v_exp_f32 v143, v143\n"
        "v_mfma_f32_32x32x16_bf16 v[160:175], a[80:83], v[208:211], 0\n"
        "buffer_load_dwordx4 a[180:183], v14, %[rs], s42 offen offset:1024 sc1\n"
        "v_exp_f32 v144, v144\n"
        "v_exp_f32 v145, v145\n"
        "v_exp_f32 v146, v146\n"
        "v_exp_f32 v147, v147\n"
        "v_mfma_f32_32x32x16_bf16 v[160:175], a[84:87], v[212:215], v[160:175]\n"
        "v_exp_f32 v148, v148\n"
        "v_exp_f32 v149, v149\n"
        "v_exp_f32 v150, v150\n"
        "v_exp_f32 v151, v151\n"
        "v_smfmac_f32_32x32x32_bf16 a[16:31], a[92:95], v[136:143], v224\n"
        "buffer_load_dwordx4 a[184:187], v14, %[rs], s42 offen offset:2048 sc1\n"
        "v_exp_f32 v152, v152\n"
        "v_exp_f32 v153, v153\n"
        "v_exp_f32 v154, v154\n"
        "v_exp_f32 v155, v155\n"
        "v_smfmac_f32_32x32x32_bf16 a[32:47], a[88:91], v[144:151], v224\n"
        "buffer_load_dwordx4 a[188:191], v14, %[rs], s42 offen offset:3072 sc1\n"
        "v_exp_f32 v156, v156\n"
        "v_exp_f32 v157, v157\n"
        "v_exp_f32 v158, v158\n"
        "v_exp_f32 v159, v159\n"
        "v_mfma_f32_32x32x16_bf16 v[176:191], a[80:83], v[216:219], 0\n"
        "buffer_load_dwordx4 a[80:83], v14, %[rs], s58 offen offset:0 sc1\n"
        "v_exp_f32 v160, v160\n"
        "v_exp_f32 v161, v161\n"
        "v_exp_f32 v162, v162\n"
        "v_exp_f32 v163, v163\n"
        "v_mfma_f32_32x32x16_bf16 v[176:191], a[84:87], v[220:223], v[176:191]\n"
        "buffer_load_dwordx4 a[84:87], v14, %[rs], s58 offen offset:1024 sc1\n"
        "v_exp_f32 v164, v164\n"
        "v_exp_f32 v165, v165\n"
        "v_exp_f32 v166, v166\n"
        "v_exp_f32 v167, v167\n"
        "v_smfmac_f32_32x32x32_bf16 a[32:47], a[92:95], v[152:159], v224\n"
        "s_load_dword s53, %[pd], 0xe40\n"
        "v_exp_f32 v168, v168\n"
        "v_exp_f32 v169, v169\n"
        "v_exp_f32 v170, v170\n"
        "v_exp_f32 v171, v171\n"
        "v_smfmac_f32_32x32x32_bf16 a[48:63], a[88:91], v[160:167], v224\n"
        "s_waitcnt vmcnt(23)\n"
        "v_exp_f32 v172, v172\n"
        "v_exp_f32 v173, v173\n"
        "v_exp_f32 v174, v174\n"
        "v_exp_f32 v175, v175\n"
        "v_mfma_f32_32x32x16_bf16 v[128:143], a[96:99], v[192:195], 0\n"
        "v_exp_f32 v176, v176\n"
        "v_exp_f32 v177, v177\n"
        "v_exp_f32 v178, v178\n"
        "v_exp_f32 v179, v179\n"
        "v_mfma_f32_32x32x16_bf16 v[128:143], a[100:103], v[196:199], v[128:143]\n"
        "buffer_load_dwordx4 a[192:195], v15, %[rs], s42 offen offset:0 sc1\n"
        "v_exp_f32 v180, v180\n"
        "v_exp_f32 v181, v181\n"
        "v_exp_f32 v182, v182\n"
        "v_exp_f32 v183, v183\n"
        "v_smfmac_f32_32x32x32_bf16 a[48:63], a[92:95], v[168:175], v224\n"
        "buffer_load_dwordx4 a[196:199], v15, %[rs], s42 offen offset:1024 sc1\n"
        "v_exp_f32 v184, v184\n"
        "v_exp_f32 v185, v185\n"
        "v_exp_f32 v186, v186\n"
        "v_exp_f32 v187, v187\n"
        "v_smfmac_f32_32x32x32_bf16 v[40:55], a[88:91], v[176:183], v224\n"
        "buffer_load_dwordx4 a[88:91], v14, %[rs], s58 offen offset:2048 sc1\n"
        "v_exp_f32 v188, v188\n"
        "v_exp_f32 v189, v189\n"
        "v_exp_f32 v190, v190\n"
        "v_exp_f32 v191, v191\n"
        "v_mfma_f32_32x32x16_bf16 v[144:159], a[96:99], v[200:203], 0\n"
        "s_load_dword s53, %[pd], 0x1180\n"
        "v_exp_f32 v128, v128\n"
        "v_exp_f32 v129, v129\n"
        "v_exp_f32 v130, v130\n"
        "v_exp_f32 v131, v131\n"
        "v_mfma_f32_32x32x16_bf16 v[144:159], a[100:103], v[204:207], v[144:159]\n"
        "v_exp_f32 v132, v132\n"
        "v_exp_f32 v133, v133\n"
        "v_exp_f32 v134, v134\n"
        "v_exp_f32 v135, v135\n"
        "v_smfmac_f32_32x32x32_bf16 v[40:55], a[92:95], v[184:191], v224\n"
        "buffer_load_dwordx4 a[92:95], v14, %[rs], s58 offen offset:3072 sc1\n"
        "v_exp_f32 v136, v136\n"
        "v_exp_f32 v137, v137\n"
        "v_exp_f32 v138, v138\n"
        "v_exp_f32 v139, v139\n"
        "v_smfmac_f32_32x32x32_bf16 a[16:31], a[104:107], v[128:135], v224\n"
        "v_exp_f32 v140, v140\n"
        "v_exp_f32 v141, v141\n"
        "v_exp_f32 v142, v142\n"
        "v_exp_f32 v143, v143\n"
        "v_mfma_f32_32x32x16_bf16 v[160:175], a[96:99], v[208:211], 0\n"
        "buffer_load_dwordx4 a[200:203], v15, %[rs], s42 offen offset:2048 sc1\n"
        "v_exp_f32 v144, v144\n"
        "v_exp_f32 v145, v145\n"
        "v_exp_f32 v146, v146\n"
        "v_exp_f32 v147, v147\n"
        "v_mfma_f32_32x32x16_bf16 v[160:175], a[100:103], v[212:215], v[160:175]\n"
        "buffer_load_dwordx4 a[204:207], v15, %[rs], s42 offen offset:3072 sc1\n"
        "v_exp_f32 v148, v148\n"
        "v_exp_f32 v149, v149\n"
        "v_exp_f32 v150, v150\n"
        "v_exp_f32 v151, v151\n"
        "v_smfmac_f32_32x32x32_bf16 a[16:31], a[108:111], v[136:143], v224\n"
        "v_exp_f32 v152, v152\n"
        "v_exp_f32 v153, v153\n"
        "v_exp_f32 v154, v154\n"
        "v_exp_f32 v155, v155\n"
        "v_smfmac_f32_32x32x32_bf16 a[32:47], a[104:107], v[144:151], v224\n"
        "buffer_load_dwordx4 a[208:211], v11, %[rs], s42 offen offset:0 sc1\n"
        "v_exp_f32 v156, v156\n"
        "v_exp_f32 v157, v157\n"
        "v_exp_f32 v158, v158\n"
        "v_exp_f32 v159, v159\n"
        "v_mfma_f32_32x32x16_bf16 v[176:191], a[96:99], v[216:219], 0\n"
        "buffer_load_dwordx4 a[96:99], v15, %[rs], s58 offen offset:0 sc1\n"
        "v_exp_f32 v160, v160\n"
        "v_exp_f32 v161, v161\n"
        "v_exp_f32 v162, v162\n"
        "v_exp_f32 v163, v163\n"
        "v_mfma_f32_32x32x16_bf16 v[176:191], a[100:103], v[220:223], v[176:191]\n"
        "buffer_load_dwordx4 a[100:103], v15, %[rs], s58 offen offset:1024 sc1\n"
        "v_exp_f32 v164, v164\n"
        "v_exp_f32 v165, v165\n"
        "v_exp_f32 v166, v166\n"
        "v_exp_f32 v167, v167\n"
        "v_smfmac_f32_32x32x32_bf16 a[32:47], a[108:111], v[152:159], v224\n"
        "s_load_dword s53, %[pd], 0x11c0\n"
        "v_exp_f32 v168, v168\n"
        "v_exp_f32 v169, v169\n"
        "v_exp_f32 v170, v170\n"
        "v_exp_f32 v171, v171\n"
        "v_smfmac_f32_32x32x32_bf16 a[48:63], a[104:107], v[160:167], v224\n"
        "s_waitcnt vmcnt(23)\n"
        "v_exp_f32 v172, v172\n"
        "v_exp_f32 v173, v173\n"
        "v_exp_f32 v174, v174\n"
        "v_exp_f32 v175, v175\n"
        "v_mfma_f32_32x32x16_bf16 v[128:143], a[112:115], v[192:195], 0\n"
        "buffer_load_dwordx4 a[212:215], v11, %[rs], s42 offen offset:1024 sc1\n"
        "v_exp_f32 v176, v176\n"
        "v_exp_f32 v177, v177\n"
        "v_exp_f32 v178, v178\n"
        "v_exp_f32 v179, v179\n"
        "v_mfma_f32_32x32x16_bf16 v[128:143], a[116:119], v[196:199], v[128:143]\n"
        "v_exp_f32 v180, v180\n"
        "v_exp_f32 v181, v181\n"
        "v_exp_f32 v182, v182\n"
        "v_exp_f32 v183, v183\n"
        "v_smfmac_f32_32x32x32_bf16 a[48:63], a[108:111], v[168:175], v224\n"
        "buffer_load_dwordx4 a[216:219], v11, %[rs], s42 offen offset:2048 sc1\n"
        "v_exp_f32 v184, v184\n"
        "v_exp_f32 v185, v185\n"
        "v_exp_f32 v186, v186\n"
        "v_exp_f32 v187, v187\n"
        "v_smfmac_f32_32x32x32_bf16 v[40:55], a[104:107], v[176:183], v224\n"
        "buffer_load_dwordx4 a[104:107], v15, %[rs], s58 offen offset:2048 sc1\n"
        "v_exp_f32 v188, v188\n"
        "v_exp_f32 v189, v189\n"
        "v_exp_f32 v190, v190\n"
        "v_exp_f32 v191, v191\n"
        "v_mfma_f32_32x32x16_bf16 v[144:159], a[112:115], v[200:203], 0\n"
        "s_load_dword s53, %[pd], 0x1500\n"
        "v_exp_f32 v128, v128\n"
        "v_exp_f32 v129, v129\n"
        "v_exp_f32 v130, v130\n"
        "v_exp_f32 v131, v131\n"
        "v_mfma_f32_32x32x16_bf16 v[144:159], a[116:119], v[204:207], v[144:159]\n"
        "v_exp_f32 v132, v132\n"
        "v_exp_f32 v133, v133\n"
        "v_exp_f32 v134, v134\n"
        "v_exp_f32 v135, v135\n"
        "v_smfmac_f32_32x32x32_bf16 v[40:55], a[108:111], v[184:191], v224\n"
        "buffer_load_dwordx4 a[108:111], v15, %[rs], s58 offen offset:3072 sc1\n"
        "v_exp_f32 v136, v136\n"
        "v_exp_f32 v137, v137\n"
        "v_exp_f32 v138, v138\n"
        "v_exp_f32 v139, v139\n"
        "v_smfmac_f32_32x32x32_bf16 a[16:31], a[120:123], v[128:135], v224\n"
        "buffer_load_dwordx4 a[220:223], v11, %[rs], s42 offen offset:3072 sc1\n"
        "v_exp_f32 v140, v140\n"
        "v_exp_f32 v141, v141\n"
        "v_exp_f32 v142, v142\n"
        "v_exp_f32 v143, v143\n"
        "v_mfma_f32_32x32x16_bf16 v[160:175], a[112:115], v[208:211], 0\n"
        "v_exp_f32 v144, v144\n"
        "v_exp_f32 v145, v145\n"
        "v_exp_f32 v146, v146\n"
        "v_exp_f32 v147, v147\n"
        "v_mfma_f32_32x32x16_bf16 v[160:175], a[116:119], v[212:215], v[160:175]\n"
        "s_add_u32 s42, s42, 0x4000\n"
        "v_exp_f32 v148, v148\n"
        "v_exp_f32 v149, v149\n"
        "v_exp_f32 v150, v150\n"
        "v_exp_f32 v151, v151\n"
        "v_smfmac_f32_32x32x32_bf16 a[16:31], a[124:127], v[136:143], v224\n"
        "s_load_dword s53, %[pd], 0x1540\n"
        "v_exp_f32 v152, v152\n"
        "v_exp_f32 v153, v153\n"
        "v_exp_f32 v154, v154\n"
        "v_exp_f32 v155, v155\n"
        "v_smfmac_f32_32x32x32_bf16 a[32:47], a[120:123], v[144:151], v224\n"
        "buffer_load_dwordx4 a[224:227], v4, %[rs], s42 offen offset:0 sc1\n"
        "v_exp_f32 v156, v156\n"
        "v_exp_f32 v157, v157\n"
        "v_exp_f32 v158, v158\n"
        "v_exp_f32 v159, v159\n"
        "v_mfma_f32_32x32x16_bf16 v[176:191], a[112:115], v[216:219], 0\n"
        "buffer_load_dwordx4 a[112:115], v11, %[rs], s58 offen offset:0 sc1\n"
        "v_exp_f32 v160, v160\n"
        "v_exp_f32 v161, v161\n"
        "v_exp_f32 v162, v162\n"
        "v_exp_f32 v163, v163\n"
        "v_mfma_f32_32x32x16_bf16 v[176:191], a[116:119], v[220:223], v[176:191]\n"
        "buffer_load_dwordx4 a[116:119], v11, %[rs], s58 offen offset:1024 sc1\n"
        "v_exp_f32 v164, v164\n"
        "v_exp_f32 v165, v165\n"
        "v_exp_f32 v166, v166\n"
        "v_exp_f32 v167, v167\n"
        "v_smfmac_f32_32x32x32_bf16 a[32:47], a[124:127], v[152:159], v224\n"
        "buffer_load_dwordx4 a[228:231], v4, %[rs], s42 offen offset:1024 sc1\n"
        "v_exp_f32 v168, v168\n"
        "v_exp_f32 v169, v169\n"
        "v_exp_f32 v170, v170\n"
        "v_exp_f32 v171, v171\n"
        "v_smfmac_f32_32x32x32_bf16 a[48:63], a[120:123], v[160:167], v224\n"
        "s_waitcnt vmcnt(23)\n"
        "v_exp_f32 v172, v172\n"
        "v_exp_f32 v173, v173\n"
        "v_exp_f32 v174, v174\n"
        "v_exp_f32 v175, v175\n"
        "v_mfma_f32_32x32x16_bf16 v[128:143], a[64:67], v[192:195], 0\n"
        "buffer_load_dwordx4 a[232:235], v4, %[rs], s42 offen offset:2048 sc1\n"
        "v_exp_f32 v176, v176\n"
        "v_exp_f32 v177, v177\n"
        "v_exp_f32 v178, v178\n"
        "v_exp_f32 v179, v179\n"
        "v_mfma_f32_32x32x16_bf16 v[128:143], a[68:71], v[196:199], v[128:143]\n"
        "s_load_dword s53, %[pd], 0x1880\n"
        "v_exp_f32 v180, v180\n"
        "v_exp_f32 v181, v181\n"
        "v_exp_f32 v182, v182\n"
        "v_exp_f32 v183, v183\n"
        "v_smfmac_f32_32x32x32_bf16 a[48:63], a[124:127], v[168:175], v224\n"
        "buffer_load_dwordx4 a[236:239], v4, %[rs], s42 offen offset:3072 sc1\n"
        "v_exp_f32 v184, v184\n"
        "v_exp_f32 v185, v185\n"
        "v_exp_f32 v186, v186\n"
        "v_exp_f32 v187, v187\n"
        "v_smfmac_f32_32x32x32_bf16 v[40:55], a[120:123], v[176:183], v224\n"
        "buffer_load_dwordx4 a[120:123], v11, %[rs], s58 offen offset:2048 sc1\n"
        "v_exp_f32 v188, v188\n"
        "v_exp_f32 v189, v189\n"
        "v_exp_f32 v190, v190\n"
        "v_exp_f32 v191, v191\n"
        "v_mfma_f32_32x32x16_bf16 v[144:159], a[64:67], v[200:203], 0\n"
        "v_exp_f32 v128, v128\n"
        "v_exp_f32 v129, v129\n"
        "v_exp_f32 v130, v130\n"
        "v_exp_f32 v131, v131\n"
        "v_mfma_f32_32x32x16_bf16 v[144:159], a[68:71], v[204:207], v[144:159]\n"
        "buffer_load_dwordx4 a[240:243], v14, %[rs], s42 offen offset:0 sc1\n"
        "v_exp_f32 v132, v132\n"
        "v_exp_f32 v133, v133\n"
        "v_exp_f32 v134, v134\n"
        "v_exp_f32 v135, v135\n"
        "v_smfmac_f32_32x32x32_bf16 v[40:55], a[124:127], v[184:191], v224\n"
        "buffer_load_dwordx4 a[124:127], v11, %[rs], s58 offen offset:3072 sc1\n"
        "v_exp_f32 v136, v136\n"
        "v_exp_f32 v137, v137\n"
        "v_exp_f32 v138, v138\n"
        "v_exp_f32 v139, v139\n"
        "v_smfmac_f32_32x32x32_bf16 a[16:31], a[72:75], v[128:135], v224\n"
        "v_exp_f32 v140, v140\n"
        "v_exp_f32 v141, v141\n"
        "v_exp_f32 v142, v142\n"
        "v_exp_f32 v143, v143\n"
        "v_mfma_f32_32x32x16_bf16 v[160:175], a[64:67], v[208:211], 0\n"
        "s_load_dword s53, %[pd], 0x18c0\n"
        "v_exp_f32 v144, v144\n"
        "v_exp_f32 v145, v145\n"
        "v_exp_f32 v146, v146\n"
        "v_exp_f32 v147, v147\n"
        "v_mfma_f32_32x32x16_bf16 v[160:175], a[68:71], v[212:215], v[160:175]\n"
        "buffer_load_dwordx4 a[244:247], v14, %[rs], s42 offen offset:1024 sc1\n"
        "v_exp_f32 v148, v148\n"
        "v_exp_f32 v149, v149\n"
        "v_exp_f32 v150, v150\n"
        "v_exp_f32 v151, v151\n"
        "v_smfmac_f32_32x32x32_bf16 a[16:31], a[76:79], v[136:143], v224\n"
        "buffer_load_dwordx4 a[248:251], v14, %[rs], s42 offen offset:2048 sc1\n"
        "v_exp_f32 v152, v152\n"
        "v_exp_f32 v153, v153\n"
        "v_exp_f32 v154, v154\n"
        "v_exp_f32 v155, v155\n"
        "v_smfmac_f32_32x32x32_bf16 a[32:47], a[72:75], v[144:151], v224\n"
        "v_exp_f32 v156, v156\n"
        "v_exp_f32 v157, v157\n"
        "v_exp_f32 v158, v158\n"
        "v_exp_f32 v159, v159\n"
        "v_mfma_f32_32x32x16_bf16 v[176:191], a[64:67], v[216:219], 0\n"
        "buffer_load_dwordx4 a[64:67], v4, %[rs], s59 offen offset:0 sc1\n"
        "v_exp_f32 v160, v160\n"
        "v_exp_f32 v161, v161\n"
        "v_exp_f32 v162, v162\n"
        "v_exp_f32 v163, v163\n"
        "v_mfma_f32_32x32x16_bf16 v[176:191], a[68:71], v[220:223], v[176:191]\n"
        "buffer_load_dwordx4 a[68:71], v4, %[rs], s59 offen offset:1024 sc1\n"
        "v_exp_f32 v164, v164\n"
        "v_exp_f32 v165, v165\n"
        "v_exp_f32 v166, v166\n"
        "v_exp_f32 v167, v167\n"
        "v_smfmac_f32_32x32x32_bf16 a[32:47], a[76:79], v[152:159], v224\n"
        "v_exp_f32 v168, v168\n"
        "v_exp_f32 v169, v169\n"
        "v_exp_f32 v170, v170\n"
        "v_exp_f32 v171, v171\n"
        "v_smfmac_f32_32x32x32_bf16 a[48:63], a[72:75], v[160:167], v224\n"
        "s_waitcnt vmcnt(23)\n"
        "v_exp_f32 v172, v172\n"
        "v_exp_f32 v173, v173\n"
        "v_exp_f32 v174, v174\n"
        "v_exp_f32 v175, v175\n"
        "v_mfma_f32_32x32x16_bf16 v[128:143], a[80:83], v[192:195], 0\n"
        "s_load_dword s53, %[pd], 0x1c00\n"
        "v_exp_f32 v176, v176\n"
        "v_exp_f32 v177, v177\n"
        "v_exp_f32 v178, v178\n"
        "v_exp_f32 v179, v179\n"
        "v_mfma_f32_32x32x16_bf16 v[128:143], a[84:87], v[196:199], v[128:143]\n"
        "buffer_load_dwordx4 a[252:255], v14, %[rs], s42 offen offset:3072 sc1\n"
        "v_exp_f32 v180, v180\n"
        "v_exp_f32 v181, v181\n"
        "v_exp_f32 v182, v182\n"
        "v_exp_f32 v183, v183\n"
        "v_smfmac_f32_32x32x32_bf16 a[48:63], a[76:79], v[168:175], v224\n"
        "buffer_load_dwordx4 a[0:3], v15, %[rs], s42 offen offset:0 sc1\n"
        "v_exp_f32 v184, v184\n"
        "v_exp_f32 v185, v185\n"
        "v_exp_f32 v186, v186\n"
        "v_exp_f32 v187, v187\n"
        "v_smfmac_f32_32x32x32_bf16 v[40:55], a[72:75], v[176:183], v224\n"
        "buffer_load_dwordx4 a[72:75], v4, %[rs], s59 offen offset:2048 sc1\n"
        "v_exp_f32 v188, v188\n"
        "v_exp_f32 v189, v189\n"
        "v_exp_f32 v190, v190\n"
        "v_exp_f32 v191, v191\n"
        "v_mfma_f32_32x32x16_bf16 v[144:159], a[80:83], v[200:203], 0\n"
        "v_exp_f32 v128, v128\n"
        "v_exp_f32 v129, v129\n"
        "v_exp_f32 v130, v130\n"
        "v_exp_f32 v131, v131\n"
        "v_mfma_f32_32x32x16_bf16 v[144:159], a[84:87], v[204:207], v[144:159]\n"
        "buffer_load_dwordx4 a[4:7], v15, %[rs], s42 offen offset:1024 sc1\n"
        "v_exp_f32 v132, v132\n"
        "v_exp_f32 v133, v133\n"
        "v_exp_f32 v134, v134\n"
        "v_exp_f32 v135, v135\n"
        "v_smfmac_f32_32x32x32_bf16 v[40:55], a[76:79], v[184:191], v224\n"
        "buffer_load_dwordx4 a[76:79], v4, %[rs], s59 offen offset:3072 sc1\n"
        "v_exp_f32 v136, v136\n"
        "v_exp_f32 v137, v137\n"
        "v_exp_f32 v138, v138\n"
        "v_exp_f32 v139, v139\n"
        "v_smfmac_f32_32x32x32_bf16 a[16:31], a[88:91], v[128:135], v224\n"
        "s_load_dword s53, %[pd], 0x1c40\n"
        "v_exp_f32 v140, v140\n"
        "v_exp_f32 v141, v141\n"
        "v_exp_f32 v142, v142\n"
        "v_exp_f32 v143, v143\n"
        "v_mfma_f32_32x32x16_bf16 v[160:175], a[80:83], v[208:211], 0\n"
        "buffer_load_dwordx4 a[8:11], v15, %[rs], s42 offen offset:2048 sc1\n"
        "v_exp_f32 v144, v144\n"
        "v_exp_f32 v145, v145\n"
        "v_exp_f32 v146, v146\n"
        "v_exp_f32 v147, v147\n"
        "v_mfma_f32_32x32x16_bf16 v[160:175], a[84:87], v[212:215], v[160:175]\n"
        "v_exp_f32 v148, v148\n"
        "v_exp_f32 v149, v149\n"
        "v_exp_f32 v150, v150\n"
        "v_exp_f32 v151, v151\n"
        "v_smfmac_f32_32x32x32_bf16 a[16:31], a[92:95], v[136:143], v224\n"
        "buffer_load_dwordx4 a[12:15], v15, %[rs], s42 offen offset:3072 sc1\n"
        "v_exp_f32 v152, v152\n"
        "v_exp_f32 v153, v153\n"
        "v_exp_f32 v154, v154\n"
        "v_exp_f32 v155, v155\n"
        "v_smfmac_f32_32x32x32_bf16 a[32:47], a[88:91], v[144:151], v224\n"
        "v_exp_f32 v156, v156\n"
        "v_exp_f32 v157, v157\n"
        "v_exp_f32 v158, v158\n"
        "v_exp_f32 v159, v159\n"
        "v_mfma_f32_32x32x16_bf16 v[176:191], a[80:83], v[216:219], 0\n"
        "buffer_load_dwordx4 a[80:83], v14, %[rs], s59 offen offset:0 sc1\n"
        "v_exp_f32 v160, v160\n"
        "v_exp_f32 v161, v161\n"
        "v_exp_f32 v162, v162\n"
        "v_exp_f32 v163, v163\n"
        "v_mfma_f32_32x32x16_bf16 v[176:191], a[84:87], v[220:223], v[176:191]\n"
        "buffer_load_dwordx4 a[84:87], v14, %[rs], s59 offen offset:1024 sc1\n"
        "v_exp_f32 v164, v164\n"
        "v_exp_f32 v165, v165\n"
        "v_exp_f32 v166, v166\n"
        "v_exp_f32 v167, v167\n"
        "v_smfmac_f32_32x32x32_bf16 a[32:47], a[92:95], v[152:159], v224\n"
        "s_load_dword s53, %[pd], 0x1f80\n"
        "v_exp_f32 v168, v168\n"
        "v_exp_f32 v169, v169\n"
        "v_exp_f32 v170, v170\n"
        "v_exp_f32 v171, v171\n"
        "v_smfmac_f32_32x32x32_bf16 a[48:63], a[88:91], v[160:167], v224\n"
        "s_waitcnt vmcnt(23)\n"
        "v_exp_f32 v172, v172\n"
        "v_exp_f32 v173, v173\n"
        "v_exp_f32 v174, v174\n"
        "v_exp_f32 v175, v175\n"
        "v_mfma_f32_32x32x16_bf16 v[128:143], a[96:99], v[192:195], 0\n"
        "buffer_load_dwordx4 v[56:59], v11, %[rs], s42 offen offset:0 sc1\n"
        "v_exp_f32 v176, v176\n"
        "v_exp_f32 v177, v177\n"
        "v_exp_f32 v178, v178\n"
        "v_exp_f32 v179, v179\n"
        "v_mfma_f32_32x32x16_bf16 v[128:143], a[100:103], v[196:199], v[128:143]\n"
        "buffer_load_dwordx4 v[60:63], v11, %[rs], s42 offen offset:1024 sc1\n"
        "v_exp_f32 v180, v180\n"
        "v_exp_f32 v181, v181\n"
        "v_exp_f32 v182, v182\n"
        "v_exp_f32 v183, v183\n"
        "v_smfmac_f32_32x32x32_bf16 a[48:63], a[92:95], v[168:175], v224\n"
        "v_exp_f32 v184, v184\n"
        "v_exp_f32 v185, v185\n"
        "v_exp_f32 v186, v186\n"
        "v_exp_f32 v187, v187\n"
        "v_smfmac_f32_32x32x32_bf16 v[40:55], a[88:91], v[176:183], v224\n"
        "buffer_load_dwordx4 a[88:91], v14, %[rs], s59 offen offset:2048 sc1\n"
        "v_exp_f32 v188, v188\n"
        "v_exp_f32 v189, v189\n"
        "v_exp_f32 v190, v190\n"
        "v_exp_f32 v191, v191\n"
        "v_mfma_f32_32x32x16_bf16 v[144:159], a[96:99], v[200:203], 0\n"
        "v_exp_f32 v128, v128\n"
        "v_exp_f32 v129, v129\n"
        "v_exp_f32 v130, v130\n"
        "v_exp_f32 v131, v131\n"
        "v_mfma_f32_32x32x16_bf16 v[144:159], a[100:103], v[204:207], v[144:159]\n"
        "s_load_dword s53, %[pd], 0x1fc0\n"
        "v_exp_f32 v132, v132\n"
        "v_exp_f32 v133, v133\n"
        "v_exp_f32 v134, v134\n"
        "v_exp_f32 v135, v135\n"
        "v_smfmac_f32_32x32x32_bf16 v[40:55], a[92:95], v[184:191], v224\n"
        "buffer_load_dwordx4 a[92:95], v14, %[rs], s59 offen offset:3072 sc1\n"
        "v_exp_f32 v136, v136\n"
        "v_exp_f32 v137, v137\n"
        "v_exp_f32 v138, v138\n"
        "v_exp_f32 v139, v139\n"
        "v_smfmac_f32_32x32x32_bf16 a[16:31], a[104:107], v[128:135], v224\n"
        "buffer_load_dwordx4 v[64:67], v11, %[rs], s42 offen offset:2048 sc1\n"
        "v_exp_f32 v140, v140\n"
        "v_exp_f32 v141, v141\n"
        "v_exp_f32 v142, v142\n"
        "v_exp_f32 v143, v143\n"
        "v_mfma_f32_32x32x16_bf16 v[160:175], a[96:99], v[208:211], 0\n"
        "buffer_load_dwordx4 v[68:71], v11, %[rs], s42 offen offset:3072 sc1\n"
        "v_exp_f32 v144, v144\n"
        "v_exp_f32 v145, v145\n"
        "v_exp_f32 v146, v146\n"
        "v_exp_f32 v147, v147\n"
        "v_mfma_f32_32x32x16_bf16 v[160:175], a[100:103], v[212:215], v[160:175]\n"
        "v_exp_f32 v148, v148\n"
        "v_exp_f32 v149, v149\n"
        "v_exp_f32 v150, v150\n"
        "v_exp_f32 v151, v151\n"
        "v_smfmac_f32_32x32x32_bf16 a[16:31], a[108:111], v[136:143], v224\n"
        "s_add_u32 s42, s42, 0x4000\n"
        "v_exp_f32 v152, v152\n"
        "v_exp_f32 v153, v153\n"
        "v_exp_f32 v154, v154\n"
        "v_exp_f32 v155, v155\n"
        "v_smfmac_f32_32x32x32_bf16 a[32:47], a[104:107], v[144:151], v224\n"
        "buffer_load_dwordx4 v[72:75], v4, %[rs], s42 offen offset:0 sc1\n"
        "v_exp_f32 v156, v156\n"
        "v_exp_f32 v157, v157\n"
        "v_exp_f32 v158, v158\n"
        "v_exp_f32 v159, v159\n"
        "v_mfma_f32_32x32x16_bf16 v[176:191], a[96:99], v[216:219], 0\n"
        "buffer_load_dwordx4 a[96:99], v15, %[rs], s59 offen offset:0 sc1\n"
        "v_exp_f32 v160, v160\n"
        "v_exp_f32 v161, v161\n"
        "v_exp_f32 v162, v162\n"
        "v_exp_f32 v163, v163\n"
        "v_mfma_f32_32x32x16_bf16 v[176:191], a[100:103], v[220:223], v[176:191]\n"
        "buffer_load_dwordx4 a[100:103], v15, %[rs], s59 offen offset:1024 sc1\n"
        "v_exp_f32 v164, v164\n"
        "v_exp_f32 v165, v165\n"
        "v_exp_f32 v166, v166\n"
        "v_exp_f32 v167, v167\n"
        "v_smfmac_f32_32x32x32_bf16 a[32:47], a[108:111], v[152:159], v224\n"
        "s_load_dword s53, %[pd], 0x2300\n"
        "v_exp_f32 v168, v168\n"
        "v_exp_f32 v169, v169\n"
        "v_exp_f32 v170, v170\n"
        "v_exp_f32 v171, v171\n"
        "v_smfmac_f32_32x32x32_bf16 a[48:63], a[104:107], v[160:167], v224\n"
        "s_waitcnt vmcnt(22)\n"
        "v_exp_f32 v172, v172\n"
        "v_exp_f32 v173, v173\n"
        "v_exp_f32 v174, v174\n"
        "v_exp_f32 v175, v175\n"
        "v_mfma_f32_32x32x16_bf16 v[128:143], a[112:115], v[192:195], 0\n"
        "buffer_load_dwordx4 v[76:79], v4, %[rs], s42 offen offset:1024 sc1\n"
        "v_exp_f32 v176, v176\n"
        "v_exp_f32 v177, v177\n"
        "v_exp_f32 v178, v178\n"
        "v_exp_f32 v179, v179\n"
        "v_mfma_f32_32x32x16_bf16 v[128:143], a[116:119], v[196:199], v[128:143]\n"
        "buffer_load_dwordx4 v[80:83], v4, %[rs], s42 offen offset:2048 sc1\n"
        "v_exp_f32 v180, v180\n"
        "v_exp_f32 v181, v181\n"
        "v_exp_f32 v182, v182\n"
        "v_exp_f32 v183, v183\n"
        "v_smfmac_f32_32x32x32_bf16 a[48:63], a[108:111], v[168:175], v224\n"
        "buffer_load_dwordx4 v[84:87], v4, %[rs], s42 offen offset:3072 sc1\n"
        "v_exp_f32 v184, v184\n"
        "v_exp_f32 v185, v185\n"
        "v_exp_f32 v186, v186\n"
        "v_exp_f32 v187, v187\n"
        "v_smfmac_f32_32x32x32_bf16 v[40:55], a[104:107], v[176:183], v224\n"
        "buffer_load_dwordx4 a[104:107], v15, %[rs], s59 offen offset:2048 sc1\n"
        "v_exp_f32 v188, v188\n"
        "v_exp_f32 v189, v189\n"
        "v_exp_f32 v190, v190\n"
        "v_exp_f32 v191, v191\n"
        "v_mfma_f32_32x32x16_bf16 v[144:159], a[112:115], v[200:203], 0\n"
        "s_load_dword s53, %[pd], 0x2340\n"
        "v_exp_f32 v128, v128\n"
        "v_exp_f32 v129, v129\n"
        "v_exp_f32 v130, v130\n"
        "v_exp_f32 v131, v131\n"
        "v_mfma_f32_32x32x16_bf16 v[144:159], a[116:119], v[204:207], v[144:159]\n"
        "v_exp_f32 v132, v132\n"
        "v_exp_f32 v133, v133\n"
        "v_exp_f32 v134, v134\n"
        "v_exp_f32 v135, v135\n"
        "v_smfmac_f32_32x32x32_bf16 v[40:55], a[108:111], v[184:191], v224\n"
        "buffer_load_dwordx4 a[108:111], v15, %[rs], s59 offen offset:3072 sc1\n"
        "v_exp_f32 v136, v136\n"
        "v_exp_f32 v137, v137\n"
        "v_exp_f32 v138, v138\n"
        "v_exp_f32 v139, v139\n"
        "v_smfmac_f32_32x32x32_bf16 a[16:31], a[120:123], v[128:135], v224\n"
        "v_exp_f32 v140, v140\n"
        "v_exp_f32 v141, v141\n"
        "v_exp_f32 v142, v142\n"
        "v_exp_f32 v143, v143\n"
        "v_mfma_f32_32x32x16_bf16 v[160:175], a[112:115], v[208:211], 0\n"
        "buffer_load_dwordx4 v[88:91], v14, %[rs], s42 offen offset:0 sc1\n"
        "v_exp_f32 v144, v144\n"
        "v_exp_f32 v145, v145\n"
        "v_exp_f32 v146, v146\n"
        "v_exp_f32 v147, v147\n"
        "v_mfma_f32_32x32x16_bf16 v[160:175], a[116:119], v[212:215], v[160:175]\n"
        "buffer_load_dwordx4 v[92:95], v14, %[rs], s42 offen offset:1024 sc1\n"
        "v_exp_f32 v148, v148\n"
        "v_exp_f32 v149, v149\n"
        "v_exp_f32 v150, v150\n"
        "v_exp_f32 v151, v151\n"
        "v_smfmac_f32_32x32x32_bf16 a[16:31], a[124:127], v[136:143], v224\n"
        "v_exp_f32 v152, v152\n"
        "v_exp_f32 v153, v153\n"
        "v_exp_f32 v154, v154\n"
        "v_exp_f32 v155, v155\n"
        "v_smfmac_f32_32x32x32_bf16 a[32:47], a[120:123], v[144:151], v224\n"
        "s_load_dword s53, %[pd], 0x2680\n"
        "v_exp_f32 v156, v156\n"
        "v_exp_f32 v157, v157\n"
        "v_exp_f32 v158, v158\n"
        "v_exp_f32 v159, v159\n"
        "v_mfma_f32_32x32x16_bf16 v[176:191], a[112:115], v[216:219], 0\n"
        "buffer_load_dwordx4 a[112:115], v11, %[rs], s59 offen offset:0 sc1\n"
        "v_exp_f32 v160, v160\n"
        "v_exp_f32 v161, v161\n"
        "v_exp_f32 v162, v162\n"
        "v_exp_f32 v163, v163\n"
        "v_mfma_f32_32x32x16_bf16 v[176:191], a[116:119], v[220:223], v[176:191]\n"
        "buffer_load_dwordx4 a[116:119], v11, %[rs], s59 offen offset:1024 sc1\n"
        "v_exp_f32 v164, v164\n"
        "v_exp_f32 v165, v165\n"
        "v_exp_f32 v166, v166\n"
        "v_exp_f32 v167, v167\n"
        "v_smfmac_f32_32x32x32_bf16 a[32:47], a[124:127], v[152:159], v224\n"
        "v_exp_f32 v168, v168\n"
        "v_exp_f32 v169, v169\n"
        "v_exp_f32 v170, v170\n"
        "v_exp_f32 v171, v171\n"
        "v_smfmac_f32_32x32x32_bf16 a[48:63], a[120:123], v[160:167], v224\n"
        "s_waitcnt vmcnt(22)\n"
        "v_exp_f32 v172, v172\n"
        "v_exp_f32 v173, v173\n"
        "v_exp_f32 v174, v174\n"
        "v_exp_f32 v175, v175\n"
        "v_mfma_f32_32x32x16_bf16 v[128:143], a[64:67], v[192:195], 0\n"
        "buffer_load_dwordx4 v[96:99], v14, %[rs], s42 offen offset:2048 sc1\n"
        "v_exp_f32 v176, v176\n"
        "v_exp_f32 v177, v177\n"
        "v_exp_f32 v178, v178\n"
        "v_exp_f32 v179, v179\n"
        "v_mfma_f32_32x32x16_bf16 v[128:143], a[68:71], v[196:199], v[128:143]\n"
        "buffer_load_dwordx4 v[100:103], v14, %[rs], s42 offen offset:3072 sc1\n"
        "v_exp_f32 v180, v180\n"
        "v_exp_f32 v181, v181\n"
        "v_exp_f32 v182, v182\n"
        "v_exp_f32 v183, v183\n"
        "v_smfmac_f32_32x32x32_bf16 a[48:63], a[124:127], v[168:175], v224\n"
        "s_load_dword s53, %[pd], 0x26c0\n"
        "v_exp_f32 v184, v184\n"
        "v_exp_f32 v185, v185\n"
        "v_exp_f32 v186, v186\n"
        "v_exp_f32 v187, v187\n"
        "v_smfmac_f32_32x32x32_bf16 v[40:55], a[120:123], v[176:183], v224\n"
        "buffer_load_dwordx4 a[120:123], v11, %[rs], s59 offen offset:2048 sc1\n"
        "v_exp_f32 v188, v188\n"
        "v_exp_f32 v189, v189\n"
        "v_exp_f32 v190, v190\n"
        "v_exp_f32 v191, v191\n"
        "v_mfma_f32_32x32x16_bf16 v[144:159], a[64:67], v[200:203], 0\n"
        "buffer_load_dwordx4 v[104:107], v15, %[rs], s42 offen offset:0 sc1\n"
        "v_exp_f32 v128, v128\n"
        "v_exp_f32 v129, v129\n"
        "v_exp_f32 v130, v130\n"
        "v_exp_f32 v131, v131\n"
        "v_mfma_f32_32x32x16_bf16 v[144:159], a[68:71], v[204:207], v[144:159]\n"
        "v_exp_f32 v132, v132\n"
        "v_exp_f32 v133, v133\n"
        "v_exp_f32 v134, v134\n"
        "v_exp_f32 v135, v135\n"
        "v_smfmac_f32_32x32x32_bf16 v[40:55], a[124:127], v[184:191], v224\n"
        "buffer_load_dwordx4 a[124:127], v11, %[rs], s59 offen offset:3072 sc1\n"
        "v_exp_f32 v136, v136\n"
        "v_exp_f32 v137, v137\n"
        "v_exp_f32 v138, v138\n"
        "v_exp_f32 v139, v139\n"
        "v_smfmac_f32_32x32x32_bf16 a[16:31], a[72:75], v[128:135], v224\n"
        "buffer_load_dwordx4 v[108:111], v15, %[rs], s42 offen offset:1024 sc1\n"
        "v_exp_f32 v140, v140\n"
        "v_exp_f32 v141, v141\n"
        "v_exp_f32 v142, v142\n"
        "v_exp_f32 v143, v143\n"
        "v_mfma_f32_32x32x16_bf16 v[160:175], a[64:67], v[208:211], 0\n"
        "v_exp_f32 v144, v144\n"
        "v_exp_f32 v145, v145\n"
        "v_exp_f32 v146, v146\n"
        "v_exp_f32 v147, v147\n"
        "v_mfma_f32_32x32x16_bf16 v[160:175], a[68:71], v[212:215], v[160:175]\n"
        "s_load_dword s53, %[pd], 0x2a00\n"
        "v_exp_f32 v148, v148\n"
        "v_exp_f32 v149, v149\n"
        "v_exp_f32 v150, v150\n"
        "v_exp_f32 v151, v151\n"
        "v_smfmac_f32_32x32x32_bf16 a[16:31], a[76:79], v[136:143], v224\n"
        "buffer_load_dwordx4 v[112:115], v15, %[rs], s42 offen offset:2048 sc1\n"
        "v_exp_f32 v152, v152\n"
        "v_exp_f32 v153, v153\n"
        "v_exp_f32 v154, v154\n"
        "v_exp_f32 v155, v155\n"
        "v_smfmac_f32_32x32x32_bf16 a[32:47], a[72:75], v[144:151], v224\n"
        "buffer_load_dwordx4 v[116:119], v15, %[rs], s42 offen offset:3072 sc1\n"
        "v_exp_f32 v156, v156\n"
        "v_exp_f32 v157, v157\n"
        "v_exp_f32 v158, v158\n"
        "v_exp_f32 v159, v159\n"
        "v_mfma_f32_32x32x16_bf16 v[176:191], a[64:67], v[216:219], 0\n"
        "v_exp_f32 v160, v160\n"
        "v_exp_f32 v161, v161\n"
        "v_exp_f32 v162, v162\n"
        "v_exp_f32 v163, v163\n"
        "v_mfma_f32_32x32x16_bf16 v[176:191], a[68:71], v[220:223], v[176:191]\n"
        "buffer_load_dwordx4 v[120:123], v11, %[rs], s42 offen offset:0 sc1\n"
        "v_exp_f32 v164, v164\n"
        "v_exp_f32 v165, v165\n"
        "v_exp_f32 v166, v166\n"
        "v_exp_f32 v167, v167\n"
        "v_smfmac_f32_32x32x32_bf16 a[32:47], a[76:79], v[152:159], v224\n"
        "buffer_load_dwordx4 v[124:127], v11, %[rs], s42 offen offset:1024 sc1\n"
        "v_exp_f32 v168, v168\n"
        "v_exp_f32 v169, v169\n"
        "v_exp_f32 v170, v170\n"
        "v_exp_f32 v171, v171\n"
        "v_smfmac_f32_32x32x32_bf16 a[48:63], a[72:75], v[160:167], v224\n"
        "s_waitcnt vmcnt(24)\n"
        "v_exp_f32 v172, v172\n"
        "v_exp_f32 v173, v173\n"
        "v_exp_f32 v174, v174\n"
        "v_exp_f32 v175, v175\n"
        "v_mfma_f32_32x32x16_bf16 v[128:143], a[80:83], v[192:195], 0\n"
        "s_load_dword s53, %[pd], 0x2a40\n"
        "v_exp_f32 v176, v176\n"
        "v_exp_f32 v177, v177\n"
        "v_exp_f32 v178, v178\n"
        "v_exp_f32 v179, v179\n"
        "v_mfma_f32_32x32x16_bf16 v[128:143], a[84:87], v[196:199], v[128:143]\n"
        "buffer_load_dwordx4 v[232:235], v11, %[rs], s42 offen offset:2048 sc1\n"
        "v_exp_f32 v180, v180\n"
        "v_exp_f32 v181, v181\n"
        "v_exp_f32 v182, v182\n"
        "v_exp_f32 v183, v183\n"
        "v_smfmac_f32_32x32x32_bf16 a[48:63], a[76:79], v[168:175], v224\n"
        "buffer_load_dwordx4 v[236:239], v11, %[rs], s42 offen offset:3072 sc1\n"
        "v_exp_f32 v184, v184\n"
        "v_exp_f32 v185, v185\n"
        "v_exp_f32 v186, v186\n"
        "v_exp_f32 v187, v187\n"
        "v_smfmac_f32_32x32x32_bf16 v[40:55], a[72:75], v[176:183], v224\n"
        "s_add_u32 s42, s42, 0x4000\n"
        "v_exp_f32 v188, v188\n"
        "v_exp_f32 v189, v189\n"
        "v_exp_f32 v190, v190\n"
        "v_exp_f32 v191, v191\n"
        "v_mfma_f32_32x32x16_bf16 v[144:159], a[80:83], v[200:203], 0\n"
        "buffer_load_dwordx4 v[240:243], v4, %[rs], s42 offen offset:0 sc1\n"
        "v_exp_f32 v128, v128\n"
        "v_exp_f32 v129, v129\n"
        "v_exp_f32 v130, v130\n"
        "v_exp_f32 v131, v131\n"
        "v_mfma_f32_32x32x16_bf16 v[144:159], a[84:87], v[204:207], v[144:159]\n"
        "buffer_load_dwordx4 v[244:247], v4, %[rs], s42 offen offset:1024 sc1\n"
        "v_exp_f32 v132, v132\n"
        "v_exp_f32 v133, v133\n"
        "v_exp_f32 v134, v134\n"
        "v_exp_f32 v135, v135\n"
        "v_smfmac_f32_32x32x32_bf16 v[40:55], a[76:79], v[184:191], v224\n"
        "v_exp_f32 v136, v136\n"
        "v_exp_f32 v137, v137\n"
        "v_exp_f32 v138, v138\n"
        "v_exp_f32 v139, v139\n"
        "v_smfmac_f32_32x32x32_bf16 a[16:31], a[88:91], v[128:135], v224\n"
        "buffer_load_dwordx4 v[248:251], v4, %[rs], s42 offen offset:2048 sc1\n"
        "v_exp_f32 v140, v140\n"
        "v_exp_f32 v141, v141\n"
        "v_exp_f32 v142, v142\n"
        "v_exp_f32 v143, v143\n"
        "v_mfma_f32_32x32x16_bf16 v[160:175], a[80:83], v[208:211], 0\n"
        "s_load_dword s53, %[pd], 0x2d80\n"
        "v_exp_f32 v144, v144\n"
        "v_exp_f32 v145, v145\n"
        "v_exp_f32 v146, v146\n"
        "v_exp_f32 v147, v147\n"
        "v_mfma_f32_32x32x16_bf16 v[160:175], a[84:87], v[212:215], v[160:175]\n"
        "buffer_load_dwordx4 v[252:255], v4, %[rs], s42 offen offset:3072 sc1\n"
        "v_exp_f32 v148, v148\n"
        "v_exp_f32 v149, v149\n"
        "v_exp_f32 v150, v150\n"
        "v_exp_f32 v151, v151\n"
        "v_smfmac_f32_32x32x32_bf16 a[16:31], a[92:95], v[136:143], v224\n"
        "buffer_load_dwordx4 a[64:67], v14, %[rs], s42 offen offset:0 sc1\n"
        "v_exp_f32 v152, v152\n"
        "v_exp_f32 v153, v153\n"
        "v_exp_f32 v154, v154\n"
        "v_exp_f32 v155, v155\n"
        "v_smfmac_f32_32x32x32_bf16 a[32:47], a[88:91], v[144:151], v224\n"
        "v_exp_f32 v156, v156\n"
        "v_exp_f32 v157, v157\n"
        "v_exp_f32 v158, v158\n"
        "v_exp_f32 v159, v159\n"
        "v_mfma_f32_32x32x16_bf16 v[176:191], a[80:83], v[216:219], 0\n"
        "buffer_load_dwordx4 a[68:71], v14, %[rs], s42 offen offset:1024 sc1\n"
        "v_exp_f32 v160, v160\n"
        "v_exp_f32 v161, v161\n"
        "v_exp_f32 v162, v162\n"
        "v_exp_f32 v163, v163\n"
        "v_mfma_f32_32x32x16_bf16 v[176:191], a[84:87], v[220:223], v[176:191]\n"
        "v_exp_f32 v164, v164\n"
        "v_exp_f32 v165, v165\n"
        "v_exp_f32 v166, v166\n"
        "v_exp_f32 v167, v167\n"
        "v_smfmac_f32_32x32x32_bf16 a[32:47], a[92:95], v[152:159], v224\n"
        "buffer_load_dwordx4 a[72:75], v14, %[rs], s42 offen offset:2048 sc1\n"
        "v_exp_f32 v168, v168\n"
        "v_exp_f32 v169, v169\n"
        "v_exp_f32 v170, v170\n"
        "v_exp_f32 v171, v171\n"
        "v_smfmac_f32_32x32x32_bf16 a[48:63], a[88:91], v[160:167], v224\n"
        "s_waitcnt vmcnt(23)\n"
        "v_exp_f32 v172, v172\n"
        "v_exp_f32 v173, v173\n"
        "v_exp_f32 v174, v174\n"
        "v_exp_f32 v175, v175\n"
        "v_mfma_f32_32x32x16_bf16 v[128:143], a[96:99], v[192:195], 0\n"
        "s_load_dword s53, %[pd], 0x2dc0\n"
        "v_exp_f32 v176, v176\n"
        "v_exp_f32 v177, v177\n"
        "v_exp_f32 v178, v178\n"
        "v_exp_f32 v179, v179\n"
        "v_mfma_f32_32x32x16_bf16 v[128:143], a[100:103], v[196:199], v[128:143]\n"
        "buffer_load_dwordx4 a[76:79], v14, %[rs], s42 offen offset:3072 sc1\n"
        "v_exp_f32 v180, v180\n"
        "v_exp_f32 v181, v181\n"
        "v_exp_f32 v182, v182\n"
        "v_exp_f32 v183, v183\n"
        "v_smfmac_f32_32x32x32_bf16 a[48:63], a[92:95], v[168:175], v224\n"
        "buffer_load_dwordx4 a[80:83], v15, %[rs], s42 offen offset:0 sc1\n"
        "v_exp_f32 v184, v184\n"
        "v_exp_f32 v185, v185\n"
        "v_exp_f32 v186, v186\n"
        "v_exp_f32 v187, v187\n"
        "v_smfmac_f32_32x32x32_bf16 v[40:55], a[88:91], v[176:183], v224\n"
        "buffer_load_dwordx4 a[84:87], v15, %[rs], s42 offen offset:1024 sc1\n"
        "v_exp_f32 v188, v188\n"
        "v_exp_f32 v189, v189\n"
        "v_exp_f32 v190, v190\n"
        "v_exp_f32 v191, v191\n"
        "v_mfma_f32_32x32x16_bf16 v[144:159], a[96:99], v[200:203], 0\n"
        "v_exp_f32 v128, v128\n"
        "v_exp_f32 v129, v129\n"
        "v_exp_f32 v130, v130\n"
        "v_exp_f32 v131, v131\n"
        "v_mfma_f32_32x32x16_bf16 v[144:159], a[100:103], v[204:207], v[144:159]\n"
        "v_exp_f32 v132, v132\n"
        "v_exp_f32 v133, v133\n"
        "v_exp_f32 v134, v134\n"
        "v_exp_f32 v135, v135\n"
        "v_smfmac_f32_32x32x32_bf16 v[40:55], a[92:95], v[184:191], v224\n"
        "buffer_load_dwordx4 a[88:91], v15, %[rs], s42 offen offset:2048 sc1\n"
        "v_exp_f32 v136, v136\n"
        "v_exp_f32 v137, v137\n"
        "v_exp_f32 v138, v138\n"
        "v_exp_f32 v139, v139\n"
        "v_smfmac_f32_32x32x32_bf16 a[16:31], a[104:107], v[128:135], v224\n"
        "buffer_load_dwordx4 a[92:95], v15, %[rs], s42 offen offset:3072 sc1\n"
        "v_exp_f32 v140, v140\n"
        "v_exp_f32 v141, v141\n"
        "v_exp_f32 v142, v142\n"
        "v_exp_f32 v143, v143\n"
        "v_mfma_f32_32x32x16_bf16 v[160:175], a[96:99], v[208:211], 0\n"
        "v_exp_f32 v144, v144\n"
        "v_exp_f32 v145, v145\n"
        "v_exp_f32 v146, v146\n"
        "v_exp_f32 v147, v147\n"
        "v_mfma_f32_32x32x16_bf16 v[160:175], a[100:103], v[212:215], v[160:175]\n"
        "v_exp_f32 v148, v148\n"
        "v_exp_f32 v149, v149\n"
        "v_exp_f32 v150, v150\n"
        "v_exp_f32 v151, v151\n"
        "v_smfmac_f32_32x32x32_bf16 a[16:31], a[108:111], v[136:143], v224\n"
        "v_exp_f32 v152, v152\n"
        "v_exp_f32 v153, v153\n"
        "v_exp_f32 v154, v154\n"
        "v_exp_f32 v155, v155\n"
        "v_smfmac_f32_32x32x32_bf16 a[32:47], a[104:107], v[144:151], v224\n"
        "v_exp_f32 v156, v156\n"
        "v_exp_f32 v157, v157\n"
        "v_exp_f32 v158, v158\n"
        "v_exp_f32 v159, v159\n"
        "v_mfma_f32_32x32x16_bf16 v[176:191], a[96:99], v[216:219], 0\n"
        "v_exp_f32 v160, v160\n"
        "v_exp_f32 v161, v161\n"
        "v_exp_f32 v162, v162\n"
        "v_exp_f32 v163, v163\n"
        "v_mfma_f32_32x32x16_bf16 v[176:191], a[100:103], v[220:223], v[176:191]\n"
        "buffer_load_dwordx4 a[96:99], v11, %[rs], s42 offen offset:0 sc1\n"
        "v_exp_f32 v164, v164\n"
        "v_exp_f32 v165, v165\n"
        "v_exp_f32 v166, v166\n"
        "v_exp_f32 v167, v167\n"
        "v_smfmac_f32_32x32x32_bf16 a[32:47], a[108:111], v[152:159], v224\n"
        "buffer_load_dwordx4 a[100:103], v11, %[rs], s42 offen offset:1024 sc1\n"
        "v_exp_f32 v168, v168\n"
        "v_exp_f32 v169, v169\n"
        "v_exp_f32 v170, v170\n"
        "v_exp_f32 v171, v171\n"
        "v_smfmac_f32_32x32x32_bf16 a[48:63], a[104:107], v[160:167], v224\n"
        "s_waitcnt vmcnt(21)\n"
        "v_exp_f32 v172, v172\n"
        "v_exp_f32 v173, v173\n"
        "v_exp_f32 v174, v174\n"
        "v_exp_f32 v175, v175\n"
        "v_mfma_f32_32x32x16_bf16 v[128:143], a[112:115], v[192:195], 0\n"
        "v_exp_f32 v176, v176\n"
        "v_exp_f32 v177, v177\n"
        "v_exp_f32 v178, v178\n"
        "v_exp_f32 v179, v179\n"
        "v_mfma_f32_32x32x16_bf16 v[128:143], a[116:119], v[196:199], v[128:143]\n"
        "buffer_load_dwordx4 v[192:195], v11, %[rs], s42 offen offset:2048 sc1\n"
        "v_exp_f32 v180, v180\n"
        "v_exp_f32 v181, v181\n"
        "v_exp_f32 v182, v182\n"
        "v_exp_f32 v183, v183\n"
        "v_smfmac_f32_32x32x32_bf16 a[48:63], a[108:111], v[168:175], v224\n"
        "buffer_load_dwordx4 v[196:199], v11, %[rs], s42 offen offset:3072 sc1\n"
        "v_exp_f32 v184, v184\n"
        "v_exp_f32 v185, v185\n"
        "v_exp_f32 v186, v186\n"
        "v_exp_f32 v187, v187\n"
        "v_smfmac_f32_32x32x32_bf16 v[40:55], a[104:107], v[176:183], v224\n"
        "v_exp_f32 v188, v188\n"
        "v_exp_f32 v189, v189\n"
        "v_exp_f32 v190, v190\n"
        "v_exp_f32 v191, v191\n"
        "v_mfma_f32_32x32x16_bf16 v[144:159], a[112:115], v[200:203], 0\n"
        "v_exp_f32 v128, v128\n"
        "v_exp_f32 v129, v129\n"
        "v_exp_f32 v130, v130\n"
        "v_exp_f32 v131, v131\n"
        "v_mfma_f32_32x32x16_bf16 v[144:159], a[116:119], v[204:207], v[144:159]\n"
        "s_add_u32 s42, s42, 0x4000\n"
        "v_exp_f32 v132, v132\n"
        "v_exp_f32 v133, v133\n"
        "v_exp_f32 v134, v134\n"
        "v_exp_f32 v135, v135\n"
        "v_smfmac_f32_32x32x32_bf16 v[40:55], a[108:111], v[184:191], v224\n"
        "buffer_load_dwordx4 v[200:203], v4, %[rs], s42 offen offset:0 sc1\n"
        "v_exp_f32 v136, v136\n"
        "v_exp_f32 v137, v137\n"
        "v_exp_f32 v138, v138\n"
        "v_exp_f32 v139, v139\n"
        "v_smfmac_f32_32x32x32_bf16 a[16:31], a[120:123], v[128:135], v224\n"
        "buffer_load_dwordx4 v[204:207], v4, %[rs], s42 offen offset:1024 sc1\n"
        "v_exp_f32 v140, v140\n"
        "v_exp_f32 v141, v141\n"
        "v_exp_f32 v142, v142\n"
        "v_exp_f32 v143, v143\n"
        "v_mfma_f32_32x32x16_bf16 v[160:175], a[112:115], v[208:211], 0\n"
        "buffer_load_dwordx4 a[104:107], v4, %[rs], s42 offen offset:2048 sc1\n"
        "v_exp_f32 v144, v144\n"
        "v_exp_f32 v145, v145\n"
        "v_exp_f32 v146, v146\n"
        "v_exp_f32 v147, v147\n"
        "v_mfma_f32_32x32x16_bf16 v[160:175], a[116:119], v[212:215], v[160:175]\n"
        "buffer_load_dwordx4 a[108:111], v4, %[rs], s42 offen offset:3072 sc1\n"
        "v_exp_f32 v148, v148\n"
        "v_exp_f32 v149, v149\n"
        "v_exp_f32 v150, v150\n"
        "v_exp_f32 v151, v151\n"
        "v_smfmac_f32_32x32x32_bf16 a[16:31], a[124:127], v[136:143], v224\n"
        "buffer_load_dwordx4 v[208:211], v14, %[rs], s42 offen offset:0 sc1\n"
        "v_exp_f32 v152, v152\n"
        "v_exp_f32 v153, v153\n"
        "v_exp_f32 v154, v154\n"
        "v_exp_f32 v155, v155\n"
        "v_smfmac_f32_32x32x32_bf16 a[32:47], a[120:123], v[144:151], v224\n"
        "buffer_load_dwordx4 v[212:215], v14, %[rs], s42 offen offset:1024 sc1\n"
        "v_exp_f32 v156, v156\n"
        "v_exp_f32 v157, v157\n"
        "v_exp_f32 v158, v158\n"
        "v_exp_f32 v159, v159\n"
        "v_mfma_f32_32x32x16_bf16 v[176:191], a[112:115], v[216:219], 0\n"
        "v_exp_f32 v160, v160\n"
        "v_exp_f32 v161, v161\n"
        "v_exp_f32 v162, v162\n"
        "v_exp_f32 v163, v163\n"
        "v_mfma_f32_32x32x16_bf16 v[176:191], a[116:119], v[220:223], v[176:191]\n"
        "ds_write_b128 v227, a[16:19] offset:0\n"
        "buffer_load_dwordx4 a[112:115], v14, %[rs], s42 offen offset:2048 sc1\n"
        "v_exp_f32 v164, v164\n"
        "v_exp_f32 v165, v165\n"
        "v_exp_f32 v166, v166\n"
        "v_exp_f32 v167, v167\n"
        "v_smfmac_f32_32x32x32_bf16 a[32:47], a[124:127], v[152:159], v224\n"
        "ds_write_b128 v227, a[20:23] offset:1024\n"
        "buffer_load_dwordx4 a[116:119], v14, %[rs], s42 offen offset:3072 sc1\n"
        "v_exp_f32 v168, v168\n"
        "v_exp_f32 v169, v169\n"
        "v_exp_f32 v170, v170\n"
        "v_exp_f32 v171, v171\n"
        "v_smfmac_f32_32x32x32_bf16 a[48:63], a[120:123], v[160:167], v224\n"
        "ds_write_b128 v227, a[24:27] offset:2048\n"
        "buffer_load_dwordx4 v[216:219], v15, %[rs], s42 offen offset:0 sc1\n"
        "v_exp_f32 v172, v172\n"
        "v_exp_f32 v173, v173\n"
        "v_exp_f32 v174, v174\n"
        "v_exp_f32 v175, v175\n"
        "ds_write_b128 v227, a[28:31] offset:3072\n"
        "buffer_load_dwordx4 v[220:223], v15, %[rs], s42 offen offset:1024 sc1\n"
        "v_exp_f32 v176, v176\n"
        "v_exp_f32 v177, v177\n"
        "v_exp_f32 v178, v178\n"
        "v_exp_f32 v179, v179\n"
        "ds_write_b128 v228, a[32:35] offset:4096\n"
        "v_exp_f32 v180, v180\n"
        "v_exp_f32 v181, v181\n"
        "v_exp_f32 v182, v182\n"
        "v_exp_f32 v183, v183\n"
        "v_smfmac_f32_32x32x32_bf16 a[48:63], a[124:127], v[168:175], v224\n"
        "ds_write_b128 v228, a[36:39] offset:5120\n"
        "v_exp_f32 v184, v184\n"
        "v_exp_f32 v185, v185\n"
        "v_exp_f32 v186, v186\n"
        "v_exp_f32 v187, v187\n"
        "v_smfmac_f32_32x32x32_bf16 v[40:55], a[120:123], v[176:183], v224\n"
        "ds_write_b128 v228, a[40:43] offset:6144\n"
        "v_exp_f32 v188, v188\n"
        "v_exp_f32 v189, v189\n"
        "v_exp_f32 v190, v190\n"
        "v_exp_f32 v191, v191\n"
        "ds_write_b128 v228, a[44:47] offset:7168\n"
        "s_nop 0\n"
        "v_smfmac_f32_32x32x32_bf16 v[40:55], a[124:127], v[184:191], v224\n"
        PROF_STAMP_ASM(6)
        "ds_write_b128 v229, a[48:51] offset:8192\n"
        "ds_write_b128 v229, a[52:55] offset:9216\n"
        "ds_write_b128 v229, a[56:59] offset:10240\n"
        "ds_write_b128 v229, a[60:63] offset:11264\n"
        "buffer_load_dwordx4 a[120:123], v15, %[rs], s42 offen offset:2048 sc1\n"
        "buffer_load_dwordx4 a[124:127], v15, %[rs], s42 offen offset:3072 sc1\n"
        "s_mov_b64 exec, s[54:55]\n"
        "global_load_lds_dwordx4 %[fs1], off\n"
        "s_mov_b64 exec, -1\n"
        "s_add_u32 m0, m0, 896\n"
        "v_lshl_add_u64 %[fs1], %[fs1], 0, s[50:51]\n"
        "s_mov_b64 exec, s[54:55]\n"
        "global_load_lds_dwordx4 %[fs1], off\n"
        "s_mov_b64 exec, -1\n"
        "s_add_u32 m0, m0, 896\n"
        "v_lshl_add_u64 %[fs1], %[fs1], 0, s[50:51]\n"
        "v_lshlrev_b32 v128, 16, v24\n"
        "v_and_b32 v129, 0xffff0000, v24\n"
        "v_lshlrev_b32 v130, 16, v25\n"
        "v_and_b32 v131, 0xffff0000, v25\n"
        "v_lshlrev_b32 v132, 16, v26\n"
        "v_and_b32 v133, 0xffff0000, v26\n"
        "v_lshlrev_b32 v134, 16, v27\n"
        "v_and_b32 v135, 0xffff0000, v27\n"
        "v_lshlrev_b32 v136, 16, v28\n"
        "v_and_b32 v137, 0xffff0000, v28\n"
        "v_lshlrev_b32 v138, 16, v29\n"
        "v_and_b32 v139, 0xffff0000, v29\n"
        "v_lshlrev_b32 v140, 16, v30\n"
        "v_and_b32 v141, 0xffff0000, v30\n"
        "v_lshlrev_b32 v142, 16, v31\n"
        "v_and_b32 v143, 0xffff0000, v31\n"
        "v_mov_b32 v225, 0x71800000\n"
        "v_mov_b32 v229, 0x7fc00000\n"
        "s_waitcnt lgkmcnt(0)\n"
        "s_barrier\n"
        "ds_read_b128 v[156:159], v226 offset:3072\n"
        "ds_read_b128 v[172:175], v226 offset:7168\n"
        "ds_read_b128 v[188:191], v226 offset:11264\n"
        "ds_read_b128 v[144:147], v226 offset:0\n"
        "ds_read_b128 v[160:163], v226 offset:4096\n"
        "ds_read_b128 v[176:179], v226 offset:8192\n"
        "ds_read_b128 v[148:151], v226 offset:1024\n"
        "ds_read_b128 v[164:167], v226 offset:5120\n"
        "ds_read_b128 v[180:183], v226 offset:9216\n"
        "ds_read_b128 v[152:155], v226 offset:2048\n"
        "ds_read_b128 v[168:171], v226 offset:6144\n"
        "ds_read_b128 v[184:187], v226 offset:10240\n"
        "s_mov_b64 exec, s[54:55]\n"
        "global_load_lds_dwordx4 %[fs1], off\n"
        "s_mov_b64 exec, -1\n"
        "s_add_u32 m0, m0, 896\n"
        "v_lshl_add_u64 %[fs1], %[fs1], 0, s[50:51]\n"
        "s_waitcnt lgkmcnt(9)\n"
        "v_pk_add_f32 v[52:53], v[52:53], v[156:157]\n"
        "v_pk_add_f32 v[54:55], v[54:55], v[158:159]\n"
        "v_pk_add_f32 v[172:173], v[172:173], v[188:189]\n"
        "v_pk_add_f32 v[174:175], v[174:175], v[190:191]\n"
        "v_pk_add_f32 v[52:53], v[52:53], v[172:173]\n"
        "v_pk_add_f32 v[54:55], v[54:55], v[174:175]\n"
        "v_rcp_f32 v228, v52\n"
        "v_cmp_gt_f32 vcc, v225, v52\n"
        "s_waitcnt lgkmcnt(6)\n"
        "v_pk_add_f32 v[40:41], v[40:41], v[144:145]\n"
        "v_pk_add_f32 v[42:43], v[42:43], v[146:147]\n"
        "v_pk_add_f32 v[160:161], v[160:161], v[176:177]\n"
        "v_pk_add_f32 v[162:163], v[162:163], v[178:179]\n"
        "v_pk_add_f32 v[40:41], v[40:41], v[160:161]\n"
        "v_pk_add_f32 v[42:43], v[42:43], v[162:163]\n"
        "v_cndmask_b32 v228, v229, v228, vcc\n"
        "v_mov_b32 v229, v228\n"
        "s_waitcnt lgkmcnt(3)\n"
        "v_pk_add_f32 v[44:45], v[44:45], v[148:149]\n"
        "v_pk_add_f32 v[46:47], v[46:47], v[150:151]\n"
        "v_pk_add_f32 v[164:165], v[164:165], v[180:181]\n"
        "v_pk_add_f32 v[166:167], v[166:167], v[182:183]\n"
        "v_pk_add_f32 v[44:45], v[44:45], v[164:165]\n"
        "v_pk_add_f32 v[46:47], v[46:47], v[166:167]\n"
        "v_permlane32_swap_b32 v229, v228\n"
        "s_waitcnt lgkmcnt(0)\n"
        "v_pk_add_f32 v[48:49], v[48:49], v[152:153]\n"
        "v_pk_add_f32 v[50:51], v[50:51], v[154:155]\n"
        "v_pk_add_f32 v[168:169], v[168:169], v[184:185]\n"
        "v_pk_add_f32 v[170:171], v[170:171], v[186:187]\n"
        "v_pk_add_f32 v[48:49], v[48:49], v[168:169]\n"
        "v_pk_add_f32 v[50:51], v[50:51], v[170:171]\n"
        "v_pk_mul_f32 v[40:41], v[40:41], v[228:229] op_sel_hi:[1,0]\n"
        "v_pk_mul_f32 v[42:43], v[42:43], v[228:229] op_sel_hi:[1,0]\n"
        "v_pk_mul_f32 v[44:45], v[44:45], v[228:229] op_sel_hi:[1,0]\n"
        "v_pk_mul_f32 v[46:47], v[46:47], v[228:229] op_sel_hi:[1,0]\n"
        "v_pk_mul_f32 v[48:49], v[48:49], v[228:229] op_sel_hi:[1,0]\n"
        "v_pk_mul_f32 v[50:51], v[50:51], v[228:229] op_sel_hi:[1,0]\n"
        "v_pk_mul_f32 v[52:53], v[52:53], v[228:229] op_sel_hi:[1,0]\n"
        "v_pk_mul_f32 v[54:55], v[54:55], v[228:229] op_sel_hi:[1,0]\n"
        "v_cvt_pk_bf16_f32 v184, v40, v41\n"
        "v_cvt_pk_bf16_f32 v185, v42, v43\n"
        "v_cvt_pk_bf16_f32 v186, v44, v45\n"
        "v_cvt_pk_bf16_f32 v187, v46, v47\n"
        "v_cvt_pk_bf16_f32 v188, v48, v49\n"
        "v_cvt_pk_bf16_f32 v189, v50, v51\n"
        "v_cvt_pk_bf16_f32 v190, v52, v53\n"
        "v_cvt_pk_bf16_f32 v191, v54, v55\n"
        "ds_write2_b32 v7, v184, v185 offset0:0 offset1:116\n"
        "ds_write2_b32 v7, v186, v187 offset0:2 offset1:118\n"
        "ds_write2_b32 v7, v188, v189 offset0:4 offset1:120\n"
        "s_mov_b32 exec_hi, 0\n"
        "ds_write2_b32 v7, v190, v191 offset0:6 offset1:122\n"
        "s_mov_b32 exec_hi, -1\n"
        "s_mov_b64 exec, s[54:55]\n"
        "global_load_lds_dwordx4 %[fs1], off\n"
        "s_mov_b64 exec, -1\n"
        "s_add_u32 m0, m0, 896\n"
        "v_lshl_add_u64 %[fs1], %[fs1], 0, s[50:51]\n"
        "v_lshlrev_b32 v144, 16, v32\n"
        "v_and_b32 v145, 0xffff0000, v32\n"
        "v_lshlrev_b32 v146, 16, v33\n"
        "v_and_b32 v147, 0xffff0000, v33\n"
        "v_lshlrev_b32 v148, 16, v34\n"
        "v_and_b32 v149, 0xffff0000, v34\n"
        "v_lshlrev_b32 v150, 16, v35\n"
        "v_and_b32 v151, 0xffff0000, v35\n"
        "v_lshlrev_b32 v152, 16, v36\n"
        "v_and_b32 v153, 0xffff0000, v36\n"
        "v_lshlrev_b32 v154, 16, v37\n"
        "v_and_b32 v155, 0xffff0000, v37\n"
        "s_waitcnt lgkmcnt(0)\n"
        "s_barrier\n"
        PROF_STAMP_ASM(7)
        "ds_read_b128 v[156:159], v8 offset:0\n"
        "ds_read_b128 v[160:163], v8 offset:64\n"
        "ds_read_b128 v[164:167], v8 offset:128\n"
        "ds_read_b128 v[168:171], v8 offset:192\n"
        "ds_read_b128 v[180:183], v8 offset:384\n"
        "ds_read_b128 v[172:175], v8 offset:256\n"
        "ds_read_b128 v[176:179], v8 offset:320\n"
        "ds_read_b64_tr_b16 a[16:17], %[wtr] offset:0\n"
        "ds_read_b64_tr_b16 a[18:19], %[wtr] offset:3584\n"
        "s_waitcnt lgkmcnt(8)\n"
        "v_smfmac_f32_16x16x64_bf16 v[128:131], v[156:159], a[128:135], v10\n"
        "ds_read_b128 a[20:23], v5 offset:0\n"
        "v_smfmac_f32_16x16x64_bf16 v[132:135], v[156:159], a[136:143], v10\n"
        "ds_read_b64_tr_b16 a[24:25], %[wtr] offset:32\n"
        "v_smfmac_f32_16x16x64_bf16 v[136:139], v[156:159], a[144:151], v10\n"
        "ds_read_b64_tr_b16 a[26:27], %[wtr] offset:3616\n"
        "v_smfmac_f32_16x16x64_bf16 v[140:143], v[156:159], a[152:159], v10\n"
        "ds_read_b128 a[28:31], v5 offset:1024\n"
        "v_smfmac_f32_16x16x64_bf16 v[144:147], v[156:159], a[160:167], v10\n"
        "ds_read_b64_tr_b16 a[32:33], %[wtr] offset:64\n"
        "v_smfmac_f32_16x16x64_bf16 v[148:151], v[156:159], a[168:175], v10\n"
        "ds_read_b64_tr_b16 a[34:35], %[wtr] offset:3648\n"
        "v_smfmac_f32_16x16x64_bf16 v[152:155], v[156:159], a[176:183], v10\n"
        "s_waitcnt lgkmcnt(6)\n"
        "v_smfmac_f32_16x16x64_bf16 v[128:131], v[160:163], a[184:191], v10\n"
        "ds_read_b128 a[36:39], v5 offset:2048\n"
        "v_smfmac_f32_16x16x64_bf16 v[132:135], v[160:163], a[192:199], v10\n"
        "ds_read_b64_tr_b16 a[40:41], %[wtr] offset:96\n"
        "v_smfmac_f32_16x16x64_bf16 v[136:139], v[160:163], a[200:207], v10\n"
        "ds_read_b64_tr_b16 a[42:43], %[wtr] offset:3680\n"
        "v_smfmac_f32_16x16x64_bf16 v[140:143], v[160:163], a[208:215], v10\n"
        "ds_read_b128 a[44:47], v5 offset:3072\n"
        "v_smfmac_f32_16x16x64_bf16 v[144:147], v[160:163], a[216:223], v10\n"
        "ds_read_b64_tr_b16 a[48:49], %[wtr] offset:128\n"
        "v_smfmac_f32_16x16x64_bf16 v[148:151], v[160:163], a[224:231], v10\n"
        "ds_read_b64_tr_b16 a[50:51], %[wtr] offset:3712\n"
        "v_smfmac_f32_16x16x64_bf16 v[152:155], v[160:163], a[232:239], v10\n"
        "ds_read_b128 a[52:55], v5 offset:4096\n"
        "v_smfmac_f32_16x16x64_bf16 v[128:131], v[164:167], a[240:247], v10\n"
        "ds_read_b64_tr_b16 a[56:57], %[wtr] offset:160\n"
        "v_smfmac_f32_16x16x64_bf16 v[132:135], v[164:167], a[248:255], v10\n"
        "s_waitcnt lgkmcnt(6)\n"
        "v_smfmac_f32_16x16x64_bf16 v[136:139], v[164:167], a[0:7], v10\n"
        "ds_read_b64_tr_b16 a[58:59], %[wtr] offset:3744\n"
        "v_smfmac_f32_16x16x64_bf16 v[140:143], v[164:167], a[8:15], v10\n"
        "ds_read_b128 a[60:63], v5 offset:5120\n"
        "v_smfmac_f32_16x16x64_bf16 v[144:147], v[164:167], v[56:63], v10\n"
        "ds_read_b64_tr_b16 v[40:41], %[wtr] offset:192\n"
        "v_smfmac_f32_16x16x64_bf16 v[148:151], v[164:167], v[64:71], v10\n"
        "ds_read_b64_tr_b16 v[42:43], %[wtr] offset:3776\n"
        "v_smfmac_f32_16x16x64_bf16 v[152:155], v[164:167], v[72:79], v10\n"
        "ds_read_b128 v[44:47], v5 offset:6144\n"
        "v_smfmac_f32_16x16x64_bf16 v[128:131], v[168:171], v[80:87], v10\n"
        "ds_read_b128 v[24:27], v6 offset:0\n"
        "v_smfmac_f32_16x16x64_bf16 v[132:135], v[168:171], v[88:95], v10\n"
        "ds_read_b128 v[28:31], v6 offset:4096\n"
        "v_smfmac_f32_16x16x64_bf16 v[136:139], v[168:171], v[96:103], v10\n"
        "ds_read_b128 v[32:35], v6 offset:8192\n"
        "s_waitcnt vmcnt(38)\n"
        "v_smfmac_f32_16x16x64_bf16 v[140:143], v[168:171], v[104:111], v10\n"
        "s_waitcnt lgkmcnt(6)\n"
        "s_waitcnt vmcnt(36)\n"
        "v_smfmac_f32_16x16x64_bf16 v[144:147], v[168:171], v[112:119], v10\n"
        "ds_read_b64 v[36:37], v6 offset:12288\n"
        "s_waitcnt vmcnt(34)\n"
        "v_smfmac_f32_16x16x64_bf16 v[148:151], v[168:171], v[120:127], v10\n"
        "s_waitcnt vmcnt(32)\n"
        "v_smfmac_f32_16x16x64_bf16 v[152:155], v[168:171], v[232:239], v10\n"
        "v_smfmac_f32_16x16x64_bf16 v[128:131], v[180:183], a[16:23], v10\n"
        "v_smfmac_f32_16x16x64_bf16 v[132:135], v[180:183], a[24:31], v10\n"
        "v_smfmac_f32_16x16x64_bf16 v[136:139], v[180:183], a[32:39], v10\n"
        "v_smfmac_f32_16x16x64_bf16 v[140:143], v[180:183], a[40:47], v10\n"
        "v_smfmac_f32_16x16x64_bf16 v[144:147], v[180:183], a[48:55], v10\n"
        "v_smfmac_f32_16x16x64_bf16 v[148:151], v[180:183], a[56:63], v10\n"
        "s_waitcnt lgkmcnt(4)\n"
        "v_smfmac_f32_16x16x64_bf16 v[152:155], v[180:183], v[40:47], v10\n"
        "s_waitcnt vmcnt(30)\n"
        "v_smfmac_f32_16x16x64_bf16 v[128:131], v[172:175], v[240:247], v10\n"
        "s_waitcnt vmcnt(28)\n"
        "v_smfmac_f32_16x16x64_bf16 v[132:135], v[172:175], v[248:255], v10\n"
        "s_waitcnt vmcnt(26)\n"
        "v_smfmac_f32_16x16x64_bf16 v[136:139], v[172:175], a[64:71], v10\n"
        "s_waitcnt vmcnt(24)\n"
        "v_smfmac_f32_16x16x64_bf16 v[140:143], v[172:175], a[72:79], v10\n"
        "s_waitcnt vmcnt(22)\n"
        "v_smfmac_f32_16x16x64_bf16 v[144:147], v[172:175], a[80:87], v10\n"
        "s_waitcnt vmcnt(20)\n"
        "v_smfmac_f32_16x16x64_bf16 v[148:151], v[172:175], a[88:95], v10\n"
        "s_waitcnt vmcnt(18)\n"
        "v_smfmac_f32_16x16x64_bf16 v[152:155], v[172:175], a[96:103], v10\n"
        "s_waitcnt vmcnt(16)\n"
        "v_smfmac_f32_16x16x64_bf16 v[128:131], v[176:179], v[192:199], v10\n"
        "s_waitcnt vmcnt(14)\n"
        "v_smfmac_f32_16x16x64_bf16 v[132:135], v[176:179], v[200:207], v10\n"
        "s_waitcnt vmcnt(12)\n"
        "v_smfmac_f32_16x16x64_bf16 v[136:139], v[176:179], a[104:111], v10\n"
        "s_waitcnt vmcnt(10)\n"
        "v_smfmac_f32_16x16x64_bf16 v[140:143], v[176:179], v[208:215], v10\n"
        "s_waitcnt vmcnt(8)\n"
        "v_smfmac_f32_16x16x64_bf16 v[144:147], v[176:179], a[112:119], v10\n"
        "s_waitcnt vmcnt(6)\n"
        "v_smfmac_f32_16x16x64_bf16 v[148:151], v[176:179], v[216:223], v10\n"
        "s_waitcnt vmcnt(4)\n"
        "v_smfmac_f32_16x16x64_bf16 v[152:155], v[176:179], a[120:127], v10\n"
        "s_waitcnt lgkmcnt(0)\n"
        "s_barrier\n"
        "s_nop 2\n"
        "v_permlane32_swap_b32 v128, v130\n"
        "v_permlane32_swap_b32 v129, v131\n"
        "v_permlane32_swap_b32 v132, v134\n"
        "v_permlane32_swap_b32 v133, v135\n"
        "v_permlane32_swap_b32 v136, v138\n"
        "v_permlane32_swap_b32 v137, v139\n"
        "v_permlane32_swap_b32 v140, v142\n"
        "v_permlane32_swap_b32 v141, v143\n"
        "v_permlane32_swap_b32 v144, v146\n"
        "v_permlane32_swap_b32 v145, v147\n"
        "v_permlane32_swap_b32 v148, v150\n"
        "v_permlane32_swap_b32 v149, v151\n"
        "v_permlane32_swap_b32 v152, v154\n"
        "v_permlane32_swap_b32 v153, v155\n"
        "v_pk_add_f32 v[128:129], v[128:129], v[130:131]\n"
        "v_pk_add_f32 v[132:133], v[132:133], v[134:135]\n"
        "v_pk_add_f32 v[136:137], v[136:137], v[138:139]\n"
        "v_pk_add_f32 v[140:141], v[140:141], v[142:143]\n"
        "v_pk_add_f32 v[144:145], v[144:145], v[146:147]\n"
        "v_pk_add_f32 v[148:149], v[148:149], v[150:151]\n"
        "v_pk_add_f32 v[152:153], v[152:153], v[154:155]\n"
        "ds_write_b32 v9, v128 offset:0\n"
        "ds_write_b32 v9, v129 offset:1808\n"
        "ds_write_b32 v9, v132 offset:64\n"
        "ds_write_b32 v9, v133 offset:1872\n"
        "ds_write_b32 v9, v136 offset:128\n"
        "ds_write_b32 v9, v137 offset:1936\n"
        "ds_write_b32 v9, v140 offset:192\n"
        "ds_write_b32 v9, v141 offset:2000\n"
        "ds_write_b32 v9, v144 offset:256\n"
        "ds_write_b32 v9, v145 offset:2064\n"
        "ds_write_b32 v9, v148 offset:320\n"
        "ds_write_b32 v9, v149 offset:2128\n"
        "ds_write_b32 v9, v152 offset:384\n"
        "ds_write_b32 v9, v153 offset:2192\n"
        "s_waitcnt lgkmcnt(0)\n"
        PROF_STAMP_ASM(8)
        "s_barrier\n"
        "ds_read_b128 v[40:43], v24\n"
        "ds_read_b128 v[44:47], v24 offset:16\n"
        "ds_read_b128 v[48:51], v24 offset:896\n"
        "ds_read_b128 v[52:55], v24 offset:912\n"
        "v_mov_b32 v65, %[tag]\n"
        "v_mov_b32 v67, %[tag]\n"
        "v_mov_b32 v69, %[tag]\n"
        "v_mov_b32 v71, %[tag]\n"
        "v_mov_b32 v73, %[tag]\n"
        "v_mov_b32 v75, %[tag]\n"
        "v_mov_b32 v77, %[tag]\n"
        "v_mov_b32 v79, %[tag]\n"
        "v_mov_b32 v88, 0\n"
        "v_mov_b32 v89, 0\n"
        "v_mov_b32 v90, 0\n"
        "s_mov_b32 exec_lo, 0x0fffffff\n"
        "s_mov_b32 exec_hi, 0x0fffffff\n"
        "s_waitcnt lgkmcnt(3)\n"
        "v_cvt_pk_bf16_f32 v64, v40, v41\n"
        "v_cvt_pk_bf16_f32 v66, v42, v43\n"
        "s_waitcnt lgkmcnt(2)\n"
        "v_cvt_pk_bf16_f32 v68, v44, v45\n"
        "v_cvt_pk_bf16_f32 v70, v46, v47\n"
        "s_waitcnt lgkmcnt(1)\n"
        "v_cvt_pk_bf16_f32 v72, v48, v49\n"
        "v_cvt_pk_bf16_f32 v74, v50, v51\n"
        "s_waitcnt lgkmcnt(0)\n"
        "v_cvt_pk_bf16_f32 v76, v52, v53\n"
        "v_cvt_pk_bf16_f32 v78, v54, v55\n"
        "v_dot2c_f32_bf16 v88, 0x3f803f80, v64\n"
        "v_dot2c_f32_bf16 v89, v64, v64\n"
        "v_dot2c_f32_bf16 v90, v64, v16\n"
        "v_dot2c_f32_bf16 v88, 0x3f803f80, v66\n"
        "v_dot2c_f32_bf16 v89, v66, v66\n"
        "v_dot2c_f32_bf16 v90, v66, v17\n"
        "v_dot2c_f32_bf16 v88, 0x3f803f80, v68\n"
        "v_dot2c_f32_bf16 v89, v68, v68\n"
        "v_dot2c_f32_bf16 v90, v68, v18\n"
        "v_dot2c_f32_bf16 v88, 0x3f803f80, v70\n"
        "v_dot2c_f32_bf16 v89, v70, v70\n"
        "v_dot2c_f32_bf16 v90, v70, v19\n"
        "v_dot2c_f32_bf16 v88, 0x3f803f80, v72\n"
        "v_dot2c_f32_bf16 v89, v72, v72\n"
        "v_dot2c_f32_bf16 v90, v72, v20\n"
        "v_dot2c_f32_bf16 v88, 0x3f803f80, v74\n"
        "v_dot2c_f32_bf16 v89, v74, v74\n"
        "v_dot2c_f32_bf16 v90, v74, v21\n"
        "v_dot2c_f32_bf16 v88, 0x3f803f80, v76\n"
        "v_dot2c_f32_bf16 v89, v76, v76\n"
        "v_dot2c_f32_bf16 v90, v76, v22\n"
        "v_dot2c_f32_bf16 v88, 0x3f803f80, v78\n"
        "v_dot2c_f32_bf16 v89, v78, v78\n"
        "v_dot2c_f32_bf16 v90, v78, v23\n"
        "s_mov_b64 exec, -1\n"
        "s_nop 2\n"
        "v_add_f32_dpp v88, v88, v88 quad_perm:[1,0,3,2] row_mask:0xf bank_mask:0xf\n"
        "v_add_f32_dpp v89, v89, v89 quad_perm:[1,0,3,2] row_mask:0xf bank_mask:0xf\n"
        "v_add_f32_dpp v90, v90, v90 quad_perm:[1,0,3,2] row_mask:0xf bank_mask:0xf\n"
        "v_add_f32_dpp v88, v88, v88 quad_perm:[2,3,0,1] row_mask:0xf bank_mask:0xf\n"
        "v_add_f32_dpp v89, v89, v89 quad_perm:[2,3,0,1] row_mask:0xf bank_mask:0xf\n"
        "v_add_f32_dpp v90, v90, v90 quad_perm:[2,3,0,1] row_mask:0xf bank_mask:0xf\n"
        "v_add_f32_dpp v88, v88, v88 row_half_mirror row_mask:0xf bank_mask:0xf\n"
        "v_add_f32_dpp v89, v89, v89 row_half_mirror row_mask:0xf bank_mask:0xf\n"
        "v_add_f32_dpp v90, v90, v90 row_half_mirror row_mask:0xf bank_mask:0xf\n"
        "v_add_f32_dpp v88, v88, v88 row_mirror row_mask:0xf bank_mask:0xf\n"
        "v_add_f32_dpp v89, v89, v89 row_mirror row_mask:0xf bank_mask:0xf\n"
        "v_add_f32_dpp v90, v90, v90 row_mirror row_mask:0xf bank_mask:0xf\n"
        "v_mov_b32 v91, v88\n"
        "v_mov_b32 v92, v89\n"
        "v_mov_b32 v93, v90\n"
        "v_permlane16_swap_b32 v91, v88\n"
        "v_permlane16_swap_b32 v92, v89\n"
        "v_permlane16_swap_b32 v93, v90\n"
        "v_add_f32 v88, v88, v91\n"
        "v_add_f32 v89, v89, v92\n"
        "v_add_f32 v90, v90, v93\n"
        "v_mul_f32 v88, 0x3b124925, v88\n"
        "v_mul_f32 v89, 0x3b124925, v89\n"
        "v_fma_f32 v89, -v88, v88, v89\n"
        "v_max_f32 v89, 0, v89\n"
        "v_cvt_pk_bf16_f32 v91, v88, v89\n"
        "v_lshlrev_b32 v80, 16, v91\n"
        "v_and_b32 v89, 0xffff0000, v91\n"
        "v_add_f32 v89, 0x3727c5ac, v89\n"
        "v_cvt_pk_bf16_f32 v89, v89, v89\n"
        "v_lshlrev_b32 v89, 16, v89\n"
        "v_sqrt_f32 v89, v89\n"
        "v_fma_f32 v90, -v80, %[swr], v90\n"
        "v_cvt_pk_bf16_f32 v93, v88, v89\n"
        "v_and_b32 v89, 0xffff0000, v93\n"
        "v_rcp_f32 v82, v89\n"
        "s_mov_b32 s40, 0xfffff800\n"
        "v_mul_f32 v90, v90, v82\n"
        "v_ashrrev_i32 v91, 31, v90\n"
        "v_cmp_class_f32 vcc, v90, %[cls]\n"
        "v_or_b32 v91, 0x80000000, v91\n"
        "v_xor_b32 v91, v91, v90\n"
        "v_cndmask_b32 v91, v91, -1, vcc\n"
        "v_and_or_b32 v92, v91, s40, v28\n"
        "s_mov_b32 exec_lo, 1\n"
        "s_mov_b32 exec_hi, 1\n"
        "ds_write2_b32 v27, v92, v93 offset1:8\n"
        "s_mov_b64 exec, -1\n"
        "v_mov_b32 v56, %[lr]\n"
        "s_waitcnt lgkmcnt(0)\n"
        "s_barrier\n"
        "ds_read_b128 v[40:43], v56\n"
        "ds_read_b128 v[44:47], v56 offset:16\n"
        "ds_read_b32 v86, v37\n"
        "s_waitcnt lgkmcnt(1)\n"
        "v_max3_u32 v48, v40, v41, v42\n"
        "v_max3_u32 v50, v43, v44, v45\n"
        "v_max_u32 v52, v46, v47\n"
        "v_med3_u32 v49, v40, v41, v42\n"
        "v_med3_u32 v51, v43, v44, v45\n"
        "v_min_u32 v53, v46, v47\n"
        "v_max3_u32 v84, v48, v50, v52\n"
        "v_med3_u32 v54, v48, v50, v52\n"
        "v_max3_u32 v55, v49, v51, v53\n"
        "v_readfirstlane_b32 %[kmax], v84\n"
        "v_max_u32 v85, v54, v55\n"
        "s_waitcnt lgkmcnt(0)\n"
        : "=&{v[16:23]}"(wrv), "=&{v[24:39]}"(tp), "=&{v[64:79]}"(ht), "=&{v[80:87]}"(cv), "+{v[4:15]}"(p2), [kmax] "=&s"(kmax), [swr] "=&s"(swr), [fs1] "+v"(fs1) PROF_ASM_OUT2
        : [rs] "s"(rs), [sq] "s"(WS_QF + wg * 8192), [sw] "s"(w), [tag] "s"(tag), [wrp] "s"(wr), [lb] "s"(lb + K2_PART), [cls] "s"(0x207), [lr] "s"(lb + K2_R),
          [wl2] "s"(lb + K2_WOL + w * 7168), [fl] "s"(lb + K2_FW1 + w * 3584), [gex] "s"(__builtin_amdgcn_readfirstlane(st1 ? -1 : 0)), [wtr] "v"(lb + K2_WOR + (8 * g + q4) * 896 + 224 * w + p4 * 8),
          [pd] "s"((const char*)wdown + (56 * (wg >> 3) + 14 * w) * 896 + min(wg & 7, 6) * 128)
        : "v40", "v41", "v42", "v43", "v44", "v45", "v46", "v47", "v48", "v49", "v50", "v51", "v52", "v53", "v54", "v55", "v56", "v57", "v58", "v59", "v60", "v61", "v62", "v63", "v88", "v89", "v90", "v91", "v92", "v93", "v94", "v95", "v96", "v97", "v98", "v99", "v100", "v101", "v102", "v103", "v104", "v105", "v106", "v107", "v108", "v109", "v110", "v111", "v112", "v113", "v114", "v115", "v116", "v117", "v118", "v119", "v120", "v121", "v122", "v123", "v124", "v125", "v126", "v127", "v128", "v129", "v130", "v131", "v132", "v133", "v134", "v135", "v136", "v137", "v138", "v139", "v140", "v141", "v142", "v143", "v144", "v145", "v146", "v147", "v148", "v149", "v150", "v151", "v152", "v153", "v154", "v155", "v156", "v157", "v158", "v159", "v160", "v161", "v162", "v163", "v164", "v165", "v166", "v167", "v168", "v169", "v170", "v171", "v172", "v173", "v174", "v175", "v176", "v177", "v178", "v179", "v180", "v181", "v182", "v183", "v184", "v185", "v186", "v187", "v188", "v189", "v190", "v191", "v192", "v193", "v194", "v195", "v196", "v197", "v198", "v199", "v200", "v201", "v202", "v203", "v204", "v205", "v206", "v207", "v208", "v209", "v210", "v211", "v212", "v213", "v214", "v215", "v216", "v217", "v218", "v219", "v220", "v221", "v222", "v223", "v224", "v225", "v226", "v227", "v228", "v229", "v230", "v231", "v232", "v233", "v234", "v235", "v236", "v237", "v238", "v239", "v240", "v241", "v242", "v243", "v244", "v245", "v246", "v247", "v248", "v249", "v250", "v251", "v252", "v253", "v254", "v255", "a0", "a1", "a2", "a3", "a4", "a5", "a6", "a7", "a8", "a9", "a10", "a11", "a12", "a13", "a14", "a15", "a16", "a17", "a18", "a19", "a20", "a21", "a22", "a23", "a24", "a25", "a26", "a27", "a28", "a29", "a30", "a31", "a32", "a33", "a34", "a35", "a36", "a37", "a38", "a39", "a40", "a41", "a42", "a43", "a44", "a45", "a46", "a47", "a48", "a49", "a50", "a51", "a52", "a53", "a54", "a55", "a56", "a57", "a58", "a59", "a60", "a61", "a62", "a63", "a64", "a65", "a66", "a67", "a68", "a69", "a70", "a71", "a72", "a73", "a74", "a75", "a76", "a77", "a78", "a79", "a80", "a81", "a82", "a83", "a84", "a85", "a86", "a87", "a88", "a89", "a90", "a91", "a92", "a93", "a94", "a95", "a96", "a97", "a98", "a99", "a100", "a101", "a102", "a103", "a104", "a105", "a106", "a107", "a108", "a109", "a110", "a111", "a112", "a113", "a114", "a115", "a116", "a117", "a118", "a119", "a120", "a121", "a122", "a123", "a124", "a125", "a126", "a127", "a128", "a129", "a130", "a131", "a132", "a133", "a134", "a135", "a136", "a137", "a138", "a139", "a140", "a141", "a142", "a143", "a144", "a145", "a146", "a147", "a148", "a149", "a150", "a151", "a152", "a153", "a154", "a155", "a156", "a157", "a158", "a159", "a160", "a161", "a162", "a163", "a164", "a165", "a166", "a167", "a168", "a169", "a170", "a171", "a172", "a173", "a174", "a175", "a176", "a177", "a178", "a179", "a180", "a181", "a182", "a183", "a184", "a185", "a186", "a187", "a188", "a189", "a190", "a191", "a192", "a193", "a194", "a195", "a196", "a197", "a198", "a199", "a200", "a201", "a202", "a203", "a204", "a205", "a206", "a207", "a208", "a209", "a210", "a211", "a212", "a213", "a214", "a215", "a216", "a217", "a218", "a219", "a220", "a221", "a222", "a223", "a224", "a225", "a226", "a227", "a228", "a229", "a230", "a231", "a232", "a233", "a234", "a235", "a236", "a237", "a238", "a239", "a240", "a241", "a242", "a243", "a244", "a245", "a246", "a247", "a248", "a249", "a250", "a251", "a252", "a253", "a254", "a255", "s40", "s41", "s42", "s43", "s44", "s45", "s46", "s47", "s48", "s49", "s50", "s51", "s52", "s53", "s54", "s55", "s56", "s57", "s58", "s59", "scc", "vcc", "memory");
    if (__builtin_expect(kmax >= 0xFFFFF800u, 0)) {
        int lane_s;
        asm volatile("s_barrier\n\tv_mbcnt_lo_u32_b32 %0, -1, 0\n\tv_mbcnt_hi_u32_b32 %0, -1, %0" : "=v"(lane_s) :: "memory");
        slow_attn(__builtin_amdgcn_make_buffer_rsrc(ws, 0, WS_END, 0x00020000), x + (size_t)t0 * D, wo, smem, wg, w, lane_s);
        asm volatile("s_waitcnt vmcnt(0) lgkmcnt(0)\n"
        "s_barrier\n"
        "ds_read_b128 v[40:43], v24\n"
        "ds_read_b128 v[44:47], v24 offset:16\n"
        "ds_read_b128 v[48:51], v24 offset:896\n"
        "ds_read_b128 v[52:55], v24 offset:912\n"
        "v_mov_b32 v65, %[tag]\n"
        "v_mov_b32 v67, %[tag]\n"
        "v_mov_b32 v69, %[tag]\n"
        "v_mov_b32 v71, %[tag]\n"
        "v_mov_b32 v73, %[tag]\n"
        "v_mov_b32 v75, %[tag]\n"
        "v_mov_b32 v77, %[tag]\n"
        "v_mov_b32 v79, %[tag]\n"
        "v_mov_b32 v88, 0\n"
        "v_mov_b32 v89, 0\n"
        "v_mov_b32 v90, 0\n"
        "s_mov_b32 exec_lo, 0x0fffffff\n"
        "s_mov_b32 exec_hi, 0x0fffffff\n"
        "s_waitcnt lgkmcnt(3)\n"
        "v_cvt_pk_bf16_f32 v64, v40, v41\n"
        "v_cvt_pk_bf16_f32 v66, v42, v43\n"
        "s_waitcnt lgkmcnt(2)\n"
        "v_cvt_pk_bf16_f32 v68, v44, v45\n"
        "v_cvt_pk_bf16_f32 v70, v46, v47\n"
        "s_waitcnt lgkmcnt(1)\n"
        "v_cvt_pk_bf16_f32 v72, v48, v49\n"
        "v_cvt_pk_bf16_f32 v74, v50, v51\n"
        "s_waitcnt lgkmcnt(0)\n"
        "v_cvt_pk_bf16_f32 v76, v52, v53\n"
        "v_cvt_pk_bf16_f32 v78, v54, v55\n"
        "v_dot2c_f32_bf16 v88, 0x3f803f80, v64\n"
        "v_dot2c_f32_bf16 v89, v64, v64\n"
        "v_dot2c_f32_bf16 v90, v64, v16\n"
        "v_dot2c_f32_bf16 v88, 0x3f803f80, v66\n"
        "v_dot2c_f32_bf16 v89, v66, v66\n"
        "v_dot2c_f32_bf16 v90, v66, v17\n"
        "v_dot2c_f32_bf16 v88, 0x3f803f80, v68\n"
        "v_dot2c_f32_bf16 v89, v68, v68\n"
        "v_dot2c_f32_bf16 v90, v68, v18\n"
        "v_dot2c_f32_bf16 v88, 0x3f803f80, v70\n"
        "v_dot2c_f32_bf16 v89, v70, v70\n"
        "v_dot2c_f32_bf16 v90, v70, v19\n"
        "v_dot2c_f32_bf16 v88, 0x3f803f80, v72\n"
        "v_dot2c_f32_bf16 v89, v72, v72\n"
        "v_dot2c_f32_bf16 v90, v72, v20\n"
        "v_dot2c_f32_bf16 v88, 0x3f803f80, v74\n"
        "v_dot2c_f32_bf16 v89, v74, v74\n"
        "v_dot2c_f32_bf16 v90, v74, v21\n"
        "v_dot2c_f32_bf16 v88, 0x3f803f80, v76\n"
        "v_dot2c_f32_bf16 v89, v76, v76\n"
        "v_dot2c_f32_bf16 v90, v76, v22\n"
        "v_dot2c_f32_bf16 v88, 0x3f803f80, v78\n"
        "v_dot2c_f32_bf16 v89, v78, v78\n"
        "v_dot2c_f32_bf16 v90, v78, v23\n"
        "s_mov_b64 exec, -1\n"
        "s_nop 2\n"
        "v_add_f32_dpp v88, v88, v88 quad_perm:[1,0,3,2] row_mask:0xf bank_mask:0xf\n"
        "v_add_f32_dpp v89, v89, v89 quad_perm:[1,0,3,2] row_mask:0xf bank_mask:0xf\n"
        "v_add_f32_dpp v90, v90, v90 quad_perm:[1,0,3,2] row_mask:0xf bank_mask:0xf\n"
        "v_add_f32_dpp v88, v88, v88 quad_perm:[2,3,0,1] row_mask:0xf bank_mask:0xf\n"
        "v_add_f32_dpp v89, v89, v89 quad_perm:[2,3,0,1] row_mask:0xf bank_mask:0xf\n"
        "v_add_f32_dpp v90, v90, v90 quad_perm:[2,3,0,1] row_mask:0xf bank_mask:0xf\n"
        "v_add_f32_dpp v88, v88, v88 row_half_mirror row_mask:0xf bank_mask:0xf\n"
        "v_add_f32_dpp v89, v89, v89 row_half_mirror row_mask:0xf bank_mask:0xf\n"
        "v_add_f32_dpp v90, v90, v90 row_half_mirror row_mask:0xf bank_mask:0xf\n"
        "v_add_f32_dpp v88, v88, v88 row_mirror row_mask:0xf bank_mask:0xf\n"
        "v_add_f32_dpp v89, v89, v89 row_mirror row_mask:0xf bank_mask:0xf\n"
        "v_add_f32_dpp v90, v90, v90 row_mirror row_mask:0xf bank_mask:0xf\n"
        "v_mov_b32 v91, v88\n"
        "v_mov_b32 v92, v89\n"
        "v_mov_b32 v93, v90\n"
        "v_permlane16_swap_b32 v91, v88\n"
        "v_permlane16_swap_b32 v92, v89\n"
        "v_permlane16_swap_b32 v93, v90\n"
        "v_add_f32 v88, v88, v91\n"
        "v_add_f32 v89, v89, v92\n"
        "v_add_f32 v90, v90, v93\n"
        "v_mul_f32 v88, 0x3b124925, v88\n"
        "v_mul_f32 v89, 0x3b124925, v89\n"
        "v_fma_f32 v89, -v88, v88, v89\n"
        "v_max_f32 v89, 0, v89\n"
        "v_cvt_pk_bf16_f32 v91, v88, v89\n"
        "v_lshlrev_b32 v80, 16, v91\n"
        "v_and_b32 v89, 0xffff0000, v91\n"
        "v_add_f32 v89, 0x3727c5ac, v89\n"
        "v_cvt_pk_bf16_f32 v89, v89, v89\n"
        "v_lshlrev_b32 v89, 16, v89\n"
        "v_sqrt_f32 v89, v89\n"
        "v_fma_f32 v90, -v80, %[swr], v90\n"
        "v_cvt_pk_bf16_f32 v93, v88, v89\n"
        "v_and_b32 v89, 0xffff0000, v93\n"
        "v_rcp_f32 v82, v89\n"
        "s_mov_b32 s40, 0xfffff800\n"
        "v_mul_f32 v90, v90, v82\n"
        "v_ashrrev_i32 v91, 31, v90\n"
        "v_cmp_class_f32 vcc, v90, %[cls]\n"
        "v_or_b32 v91, 0x80000000, v91\n"
        "v_xor_b32 v91, v91, v90\n"
        "v_cndmask_b32 v91, v91, -1, vcc\n"
        "v_and_or_b32 v92, v91, s40, v28\n"
        "s_mov_b32 exec_lo, 1\n"
        "s_mov_b32 exec_hi, 1\n"
        "ds_write2_b32 v27, v92, v93 offset1:8\n"
        "s_mov_b64 exec, -1\n"
        "v_mov_b32 v56, %[lr]\n"
        "s_waitcnt lgkmcnt(0)\n"
        "s_barrier\n"
        "ds_read_b128 v[40:43], v56\n"
        "ds_read_b128 v[44:47], v56 offset:16\n"
        "ds_read_b32 v86, v37\n"
        "s_waitcnt lgkmcnt(1)\n"
        "v_max3_u32 v48, v40, v41, v42\n"
        "v_max3_u32 v50, v43, v44, v45\n"
        "v_max_u32 v52, v46, v47\n"
        "v_med3_u32 v49, v40, v41, v42\n"
        "v_med3_u32 v51, v43, v44, v45\n"
        "v_min_u32 v53, v46, v47\n"
        "v_max3_u32 v84, v48, v50, v52\n"
        "v_med3_u32 v54, v48, v50, v52\n"
        "v_max3_u32 v55, v49, v51, v53\n"
        "v_readfirstlane_b32 %[kmax], v84\n"
        "v_max_u32 v85, v54, v55\n"
        "s_waitcnt lgkmcnt(0)\n"
        : "=&{v[64:79]}"(ht), "=&{v[80:87]}"(cv), [kmax] "=&s"(kmax)
        : "{v[16:23]}"(wrv), "{v[24:39]}"(tp), [rs] "s"(rs), [tag] "s"(tag), [swr] "s"(swr), [cls] "s"(0x207), [lr] "s"(lb + K2_R)
        : "v40", "v41", "v42", "v43", "v44", "v45", "v46", "v47", "v48", "v49", "v50", "v51", "v52", "v53", "v54", "v55", "v56", "v57", "v58", "v59", "v60", "v61", "v62", "v63", "v88", "v89", "v90", "v91", "v92", "v93", "v94", "v95", "s40", "scc", "vcc", "memory");
    }
    STAMP(9);
    asm volatile(
        "v_mov_b32 v87, %[tag]\n"
        "s_cmp_lg_u32 %[sw], 0\n"
        "s_cbranch_scc1 .Lnp%=\n"
        "s_mov_b64 vcc, 1\n"
        "v_cndmask_b32 v86, v86, v84, vcc\n"
        "s_mov_b64 vcc, 2\n"
        "v_cndmask_b32 v86, v86, v85, vcc\n"
        "v_lshrrev_b32 v88, 1, v4\n"
        "s_lshr_b32 s40, %[sq], 6\n"
        "s_add_u32 s40, s40, 2764800\n"
        "s_mov_b64 exec, 0x3ff\n"
        "buffer_store_dwordx2 v[86:87], v88, %[rs], s40 offen sc1\n"
        "s_mov_b64 exec, -1\n"
        ".Lnp%=:\n"
        "v_and_b32 v88, 0x7ff, v84\n"
        "v_and_b32 v89, 0x7ff, v85\n"
        "v_cmp_eq_u32 vcc, v88, v28\n"
        "v_cmp_eq_u32 s[46:47], v89, v28\n"
        "s_or_b64 s[56:57], vcc, s[46:47]\n"
        "s_and_b32 exec_lo, s56, 0x0fffffff\n"
        "s_and_b32 exec_hi, s57, 0x0fffffff\n"
        "buffer_store_dwordx4 v[64:67], v25, %[rs], 0 offen offset:0 sc1\n"
        "buffer_store_dwordx4 v[68:71], v25, %[rs], 0 offen offset:448 sc1\n"
        "buffer_store_dwordx4 v[72:75], v25, %[rs], 0 offen offset:896 sc1\n"
        "buffer_store_dwordx4 v[76:79], v25, %[rs], 0 offen offset:1344 sc1\n"
        "s_mov_b64 exec, -1\n"
        PROF_STAMP_ASM(10)
        "s_cmp_lg_u32 %[sw], 0\n"
        "s_cbranch_scc1 .Lout%=\n"
        "s_mov_b32 s40, 0x10000\n"
        "v_lshlrev_b32 v90, 3, v4\n"
        "s_mov_b32 s54, 2764800\n"
        "s_mov_b32 s41, 2772992\n"
        "s_mov_b32 s42, 2781184\n"
        "s_mov_b32 s43, 2789376\n"
        "s_mov_b64 s[44:45], -1\n"
        "s_mov_b64 s[46:47], -1\n"
        "s_mov_b64 s[48:49], -1\n"
        "s_mov_b64 s[50:51], -1\n"
        "v_mov_b32 v118, %[lr]\n"
        "s_cmp_eq_u32 %[role], 1\n"
        "s_cselect_b32 s58, 0, 0xffff0000\n"
        "s_cselect_b32 s59, -1, 0xffff0000\n"
        ".Lsw%=:\n"
        "s_mov_b64 exec, s[44:45]\n"
        "buffer_load_dwordx4 v[96:99], v90, %[rs], s54 offen offset:0 sc1\n"
        "s_mov_b64 exec, s[46:47]\n"
        "buffer_load_dwordx4 v[100:103], v90, %[rs], s41 offen offset:0 sc1\n"
        "s_mov_b64 exec, s[48:49]\n"
        "buffer_load_dwordx4 v[104:107], v90, %[rs], s42 offen offset:0 sc1\n"
        "s_mov_b64 exec, s[50:51]\n"
        "buffer_load_dwordx4 v[108:111], v90, %[rs], s43 offen offset:0 sc1\n"
        "s_mov_b64 exec, -1\n"
        "s_sub_u32 s40, s40, 1\n"
        "s_waitcnt vmcnt(3)\n"
        "v_cmp_ne_u32 s[44:45], %[tag], v97\n"
        "v_cmp_ne_u32 vcc, %[tag], v99\n"
        "s_or_b64 s[44:45], s[44:45], vcc\n"
        "s_waitcnt vmcnt(2)\n"
        "v_cmp_ne_u32 s[46:47], %[tag], v101\n"
        "v_cmp_ne_u32 vcc, %[tag], v103\n"
        "s_or_b64 s[46:47], s[46:47], vcc\n"
        "s_waitcnt vmcnt(1)\n"
        "v_cmp_ne_u32 s[48:49], %[tag], v105\n"
        "v_cmp_ne_u32 vcc, %[tag], v107\n"
        "s_or_b64 s[48:49], s[48:49], vcc\n"
        "s_waitcnt vmcnt(0)\n"
        "v_cmp_ne_u32 s[50:51], %[tag], v109\n"
        "v_cmp_ne_u32 vcc, %[tag], v111\n"
        "s_or_b64 s[50:51], s[50:51], vcc\n"
        "s_cmp_eq_u32 s40, 0\n"
        "s_cbranch_scc1 .Lswd%=\n"
        "s_or_b64 s[52:53], s[44:45], s[46:47]\n"
        "s_or_b64 vcc, s[48:49], s[50:51]\n"
        "s_or_b64 s[52:53], s[52:53], vcc\n"
        "s_cbranch_scc1 .Lsw%=\n"
        ".Lswd%=:\n"
        "v_max3_u32 v112, v96, v100, v104\n"
        "v_med3_u32 v113, v96, v100, v104\n"
        "v_max3_u32 v114, v98, v102, v106\n"
        "v_min_u32 v115, v112, v108\n"
        "v_max_u32 v112, v112, v108\n"
        "v_max3_u32 v113, v113, v114, v110\n"
        "v_mov_b32 v116, v112\n"
        "v_max_u32 v113, v113, v115\n"
        "s_nop 1\n"
        "v_max_u32_dpp v116, v116, v116 quad_perm:[1,0,3,2] row_mask:0xf bank_mask:0xf\n"
        "s_nop 1\n"
        "v_max_u32_dpp v116, v116, v116 quad_perm:[2,3,0,1] row_mask:0xf bank_mask:0xf\n"
        "s_nop 1\n"
        "v_max_u32_dpp v116, v116, v116 row_half_mirror row_mask:0xf bank_mask:0xf\n"
        "s_nop 1\n"
        "v_max_u32_dpp v116, v116, v116 row_mirror row_mask:0xf bank_mask:0xf\n"
        "v_mov_b32 v117, v116\n"
        "s_nop 1\n"
        "v_permlane16_swap_b32 v117, v116\n"
        "v_max_u32 v116, v116, v117\n"
        "v_mov_b32 v117, v116\n"
        "s_nop 1\n"
        "v_permlane32_swap_b32 v117, v116\n"
        "v_max_u32 v116, v116, v117\n"
        "v_cmp_eq_u32 vcc, v112, v116\n"
        "s_nop 1\n"
        "v_cndmask_b32 v117, v112, v113, vcc\n"
        "s_nop 1\n"
        "v_max_u32_dpp v117, v117, v117 quad_perm:[1,0,3,2] row_mask:0xf bank_mask:0xf\n"
        "s_nop 1\n"
        "v_max_u32_dpp v117, v117, v117 quad_perm:[2,3,0,1] row_mask:0xf bank_mask:0xf\n"
        "s_nop 1\n"
        "v_max_u32_dpp v117, v117, v117 row_half_mirror row_mask:0xf bank_mask:0xf\n"
        "s_nop 1\n"
        "v_max_u32_dpp v117, v117, v117 row_mirror row_mask:0xf bank_mask:0xf\n"
        "v_mov_b32 v119, v117\n"
        "s_nop 1\n"
        "v_permlane16_swap_b32 v119, v117\n"
        "v_max_u32 v117, v117, v119\n"
        "v_mov_b32 v119, v117\n"
        "s_nop 1\n"
        "v_permlane32_swap_b32 v119, v117\n"
        "v_max_u32 v117, v117, v119\n"
        "ds_write_b64 v118, v[116:117] offset:64\n"
        "v_cndmask_b32 v120, v116, v117, s[58:59]\n"
        "v_not_b32 v119, v120\n"
        "v_and_b32 v119, 0x7ff, v119\n"
        "v_mad_u32_u24 v124, v119, %[c1792], v30\n"
        "v_lshrrev_b32 v121, 6, v119\n"
        "v_bfe_u32 v122, v119, 3, 3\n"
        "v_lshl_or_b32 v121, v122, 5, v121\n"
        "v_and_b32 v122, 7, v119\n"
        "v_lshlrev_b32 v122, 3, v122\n"
        "v_lshl_add_u32 v125, v121, 7, v122\n"
        PROF_STAMP_ASM(11)
        "s_mov_b32 s40, 0x10000\n"
        ".Lrow%=:\n"
        "buffer_load_dwordx4 v[96:99], v124, %[rs], %[hrow] offen offset:0 sc1\n"
        "buffer_load_dwordx4 v[100:103], v124, %[rs], %[hrow] offen offset:448 sc1\n"
        "buffer_load_dwordx4 v[104:107], v124, %[rs], %[hrow] offen offset:896 sc1\n"
        "buffer_load_dwordx4 v[108:111], v124, %[rs], %[hrow] offen offset:1344 sc1\n"
        "buffer_load_dwordx2 v[122:123], v125, %[rs], s54 offen offset:16 sc1\n"
        "s_cmp_lg_u32 s40, 0x10000\n"
        "s_cbranch_scc1 .Lrchk%=\n"
        "s_cmp_lg_u32 %[sq], 0x1fe000\n"
        "s_cbranch_scc1 .Lwb0%=\n"
        "s_mov_b32 s41, 2830336\n"
        "s_mov_b64 exec, 1\n"
        "buffer_store_dword v87, off, %[rs], s41 sc1\n"
        "s_mov_b64 exec, -1\n"
        ".Lwb0%=:\n"
        "s_waitcnt lgkmcnt(0)\n"
        "s_barrier\n"
        "s_cmp_lg_u32 %[role], 1\n"
        "s_cbranch_scc1 .Lout%=\n"
        "ds_read_b64_tr_b16 v[128:129], v33 offset:0\n"
        "ds_read_b64_tr_b16 v[130:131], v33 offset:128\n"
        "ds_read_b64_tr_b16 v[132:133], v33 offset:1024\n"
        "ds_read_b64_tr_b16 v[134:135], v33 offset:1152\n"
        "ds_read_b64_tr_b16 v[136:137], v33 offset:2048\n"
        "ds_read_b64_tr_b16 v[138:139], v33 offset:2176\n"
        "ds_read_b64_tr_b16 v[140:141], v33 offset:3072\n"
        "ds_read_b64_tr_b16 v[142:143], v33 offset:3200\n"
        "ds_read_b64_tr_b16 v[144:145], v33 offset:4096\n"
        "ds_read_b64_tr_b16 v[146:147], v33 offset:4224\n"
        "ds_read_b64_tr_b16 v[148:149], v33 offset:5120\n"
        "ds_read_b64_tr_b16 v[150:151], v33 offset:5248\n"
        "ds_read_b64_tr_b16 v[152:153], v33 offset:6144\n"
        "ds_read_b64_tr_b16 v[154:155], v33 offset:6272\n"
        "ds_read_b64_tr_b16 v[156:157], v33 offset:7168\n"
        "ds_read_b64_tr_b16 v[158:159], v33 offset:7296\n"
        "ds_read_b64_tr_b16 v[160:161], v33 offset:8192\n"
        "ds_read_b64_tr_b16 v[162:163], v33 offset:8320\n"
        "ds_read_b64_tr_b16 v[164:165], v33 offset:9216\n"
        "ds_read_b64_tr_b16 v[166:167], v33 offset:9344\n"
        "ds_read_b64_tr_b16 v[168:169], v33 offset:10240\n"
        "ds_read_b64_tr_b16 v[170:171], v33 offset:10368\n"
        "ds_read_b64_tr_b16 v[172:173], v33 offset:11264\n"
        "ds_read_b64_tr_b16 v[174:175], v33 offset:11392\n"
        "ds_read_b64_tr_b16 v[176:177], v33 offset:12288\n"
        "ds_read_b64_tr_b16 v[178:179], v33 offset:12416\n"
        "ds_read_b64_tr_b16 v[180:181], v33 offset:13312\n"
        "ds_read_b64_tr_b16 v[182:183], v33 offset:13440\n"
        ".Lrchk%=:\n"
        "s_waitcnt vmcnt(0)\n"
        "v_and_b32 v112, v97, v99\n"
        "v_or_b32 v113, v97, v99\n"
        "v_bitop3_b32 v112, v112, v101, v103 bitop3:0x80\n"
        "v_or3_b32 v113, v113, v101, v103\n"
        "v_bitop3_b32 v112, v112, v105, v107 bitop3:0x80\n"
        "v_or3_b32 v113, v113, v105, v107\n"
        "v_bitop3_b32 v112, v112, v109, v111 bitop3:0x80\n"
        "v_or3_b32 v113, v113, v109, v111\n"
        "v_and_b32 v112, v112, v123\n"
        "v_or_b32 v113, v113, v123\n"
        "v_cmp_ne_u32 vcc, %[tag], v112\n"
        "v_cmp_ne_u32 s[46:47], %[tag], v113\n"
        "s_or_b64 vcc, vcc, s[46:47]\n"
        "s_cbranch_scc0 .Lrok%=\n"
        "s_sub_u32 s40, s40, 1\n"
        "s_cmp_lg_u32 s40, 0\n"
        "s_cbranch_scc1 .Lrow%=\n"
        ".Lrok%=:\n"
        "ds_write2_b32 v31, v96, v98 offset0:0 offset1:1\n"
        "ds_write2_b32 v31, v100, v102 offset0:2 offset1:3\n"
        "ds_write2_b32 v31, v104, v106 offset0:112 offset1:113\n"
        "ds_write2_b32 v31, v108, v110 offset0:114 offset1:115\n"
        "ds_read_b128 v[184:187], v32 offset:0\n"
        "ds_read_b128 v[188:191], v32 offset:64\n"
        "ds_read_b128 v[192:195], v32 offset:128\n"
        "ds_read_b128 v[196:199], v32 offset:192\n"
        "ds_read_b128 v[200:203], v32 offset:256\n"
        "ds_read_b128 v[204:207], v32 offset:320\n"
        "ds_read_b128 v[208:211], v32 offset:384\n"
        "ds_read_b128 v[212:215], v32 offset:448\n"
        "ds_read_b128 v[216:219], v32 offset:512\n"
        "ds_read_b128 v[220:223], v32 offset:576\n"
        "ds_read_b128 v[224:227], v32 offset:640\n"
        "ds_read_b128 v[228:231], v32 offset:704\n"
        "ds_read_b128 v[232:235], v32 offset:768\n"
        "ds_read_b128 v[236:239], v32 offset:832\n"
        "v_readlane_b32 s46, v122, 0\n"
        "v_readlane_b32 s47, v122, 32\n"
        "s_lshl_b32 s48, s46, 16\n"
        "s_lshl_b32 s49, s47, 16\n"
        "s_and_b32 s46, s46, 0xffff0000\n"
        "s_and_b32 s47, s47, 0xffff0000\n"
        "v_rcp_f32 v112, s46\n"
        "v_rcp_f32 v113, s47\n"
        PROF_STAMP_ASM(12)
        "s_waitcnt lgkmcnt(12)\n"
        "v_mfma_f32_16x16x32_bf16 v[240:243], v[184:187], v[128:131], 0\n"
        "v_mfma_f32_16x16x32_bf16 v[244:247], v[188:191], v[132:135], 0\n"
        "s_waitcnt lgkmcnt(10)\n"
        "v_mfma_f32_16x16x32_bf16 v[240:243], v[192:195], v[136:139], v[240:243]\n"
        "v_mfma_f32_16x16x32_bf16 v[244:247], v[196:199], v[140:143], v[244:247]\n"
        "s_waitcnt lgkmcnt(8)\n"
        "v_mfma_f32_16x16x32_bf16 v[240:243], v[200:203], v[144:147], v[240:243]\n"
        "v_mfma_f32_16x16x32_bf16 v[244:247], v[204:207], v[148:151], v[244:247]\n"
        "s_waitcnt lgkmcnt(6)\n"
        "v_mfma_f32_16x16x32_bf16 v[240:243], v[208:211], v[152:155], v[240:243]\n"
        "v_mfma_f32_16x16x32_bf16 v[244:247], v[212:215], v[156:159], v[244:247]\n"
        "s_waitcnt lgkmcnt(4)\n"
        "v_mfma_f32_16x16x32_bf16 v[240:243], v[216:219], v[160:163], v[240:243]\n"
        "v_mfma_f32_16x16x32_bf16 v[244:247], v[220:223], v[164:167], v[244:247]\n"
        "s_waitcnt lgkmcnt(2)\n"
        "v_mfma_f32_16x16x32_bf16 v[240:243], v[224:227], v[168:171], v[240:243]\n"
        "v_mfma_f32_16x16x32_bf16 v[244:247], v[228:231], v[172:175], v[244:247]\n"
        "s_waitcnt lgkmcnt(0)\n"
        "v_mfma_f32_16x16x32_bf16 v[240:243], v[232:235], v[176:179], v[240:243]\n"
        "v_mfma_f32_16x16x32_bf16 v[244:247], v[236:239], v[180:183], v[244:247]\n"
        "s_nop 7\n"
        "v_pk_add_f32 v[240:241], v[240:241], v[244:245]\n"
        "v_add_f32 v242, v242, v246\n"
        "v_fma_f32 v240, -v242, s48, v240\n"
        "v_fma_f32 v241, -v242, s49, v241\n"
        "v_pk_mul_f32 v[240:241], v[240:241], v[112:113]\n"
        "v_cvt_pk_bf16_f32 v242, v240, v241\n"
        "v_lshlrev_b32 v240, 16, v242\n"
        "v_and_b32 v241, 0xffff0000, v242\n"
        "v_mul_f32 v244, 0xbfb8aa3b, v240\n"
        "v_mul_f32 v245, 0xbfb8aa3b, v241\n"
        "v_exp_f32 v244, v244\n"
        "v_exp_f32 v245, v245\n"
        "v_mov_b32_dpp v246, v240 row_ror:8 row_mask:0xf bank_mask:0xf\n"
        "v_mov_b32_dpp v247, v241 row_ror:8 row_mask:0xf bank_mask:0xf\n"
        "v_add_f32 v244, 1.0, v244\n"
        "v_add_f32 v245, 1.0, v245\n"
        "v_rcp_f32 v244, v244\n"
        "v_rcp_f32 v245, v245\n"
        "s_nop 0\n"
        "v_pk_mul_f32 v[240:241], v[240:241], v[244:245]\n"
        "v_cvt_pk_bf16_f32 v242, v240, v241\n"
        "v_lshlrev_b32 v240, 16, v242\n"
        "v_and_b32 v241, 0xffff0000, v242\n"
        "v_pk_mul_f32 v[240:241], v[240:241], v[246:247]\n"
        "v_cvt_pk_bf16_f32 v86, v240, v241\n"
        "s_mov_b64 exec, 0xff\n"
        "buffer_store_dwordx2 v[86:87], v34, %[rs], 0 offen sc1\n"
        "s_mov_b64 exec, -1\n"
        PROF_STAMP_ASM(13)
        ".Lout%=:\n"
        "v_lshlrev_b32 v40, 16, v64\n"
        "v_and_b32 v41, 0xffff0000, v64\n"
        "v_lshlrev_b32 v42, 16, v66\n"
        "v_and_b32 v43, 0xffff0000, v66\n"
        "v_lshlrev_b32 v44, 16, v68\n"
        "v_and_b32 v45, 0xffff0000, v68\n"
        "v_lshlrev_b32 v46, 16, v70\n"
        "v_and_b32 v47, 0xffff0000, v70\n"
        "v_lshlrev_b32 v48, 16, v72\n"
        "v_and_b32 v49, 0xffff0000, v72\n"
        "v_lshlrev_b32 v50, 16, v74\n"
        "v_and_b32 v51, 0xffff0000, v74\n"
        "v_lshlrev_b32 v52, 16, v76\n"
        "v_and_b32 v53, 0xffff0000, v76\n"
        "v_lshlrev_b32 v54, 16, v78\n"
        "v_and_b32 v55, 0xffff0000, v78\n"
        "v_pk_add_f32 v[128:129], v[40:41], v[80:81] op_sel_hi:[1,0] neg_lo:[0,1] neg_hi:[0,1]\n"
        "v_pk_add_f32 v[130:131], v[42:43], v[80:81] op_sel_hi:[1,0] neg_lo:[0,1] neg_hi:[0,1]\n"
        "v_pk_add_f32 v[132:133], v[44:45], v[80:81] op_sel_hi:[1,0] neg_lo:[0,1] neg_hi:[0,1]\n"
        "v_pk_add_f32 v[134:135], v[46:47], v[80:81] op_sel_hi:[1,0] neg_lo:[0,1] neg_hi:[0,1]\n"
        "v_pk_add_f32 v[136:137], v[48:49], v[80:81] op_sel_hi:[1,0] neg_lo:[0,1] neg_hi:[0,1]\n"
        "v_pk_add_f32 v[138:139], v[50:51], v[80:81] op_sel_hi:[1,0] neg_lo:[0,1] neg_hi:[0,1]\n"
        "v_pk_add_f32 v[140:141], v[52:53], v[80:81] op_sel_hi:[1,0] neg_lo:[0,1] neg_hi:[0,1]\n"
        "v_pk_add_f32 v[142:143], v[54:55], v[80:81] op_sel_hi:[1,0] neg_lo:[0,1] neg_hi:[0,1]\n"
        "v_cvt_pk_bf16_f32 v56, v128, v129\n"
        "v_cvt_pk_bf16_f32 v57, v130, v131\n"
        "v_cvt_pk_bf16_f32 v58, v132, v133\n"
        "v_cvt_pk_bf16_f32 v59, v134, v135\n"
        "v_cvt_pk_bf16_f32 v60, v136, v137\n"
        "v_cvt_pk_bf16_f32 v61, v138, v139\n"
        "v_cvt_pk_bf16_f32 v62, v140, v141\n"
        "v_cvt_pk_bf16_f32 v63, v142, v143\n"
        "v_lshlrev_b32 v128, 16, v56\n"
        "v_and_b32 v129, 0xffff0000, v56\n"
        "v_lshlrev_b32 v130, 16, v57\n"
        "v_and_b32 v131, 0xffff0000, v57\n"
        "v_lshlrev_b32 v132, 16, v58\n"
        "v_and_b32 v133, 0xffff0000, v58\n"
        "v_lshlrev_b32 v134, 16, v59\n"
        "v_and_b32 v135, 0xffff0000, v59\n"
        "v_lshlrev_b32 v136, 16, v60\n"
        "v_and_b32 v137, 0xffff0000, v60\n"
        "v_lshlrev_b32 v138, 16, v61\n"
        "v_and_b32 v139, 0xffff0000, v61\n"
        "v_lshlrev_b32 v140, 16, v62\n"
        "v_and_b32 v141, 0xffff0000, v62\n"
        "v_lshlrev_b32 v142, 16, v63\n"
        "v_and_b32 v143, 0xffff0000, v63\n"
        "v_pk_mul_f32 v[128:129], v[128:129], v[82:83] op_sel_hi:[1,0]\n"
        "v_pk_mul_f32 v[130:131], v[130:131], v[82:83] op_sel_hi:[1,0]\n"
        "v_pk_mul_f32 v[132:133], v[132:133], v[82:83] op_sel_hi:[1,0]\n"
        "v_pk_mul_f32 v[134:135], v[134:135], v[82:83] op_sel_hi:[1,0]\n"
        "v_pk_mul_f32 v[136:137], v[136:137], v[82:83] op_sel_hi:[1,0]\n"
        "v_pk_mul_f32 v[138:139], v[138:139], v[82:83] op_sel_hi:[1,0]\n"
        "v_pk_mul_f32 v[140:141], v[140:141], v[82:83] op_sel_hi:[1,0]\n"
        "v_pk_mul_f32 v[142:143], v[142:143], v[82:83] op_sel_hi:[1,0]\n"
        "v_cvt_pk_bf16_f32 v56, v128, v129\n"
        "v_cvt_pk_bf16_f32 v57, v130, v131\n"
        "v_cvt_pk_bf16_f32 v58, v132, v133\n"
        "v_cvt_pk_bf16_f32 v59, v134, v135\n"
        "v_cvt_pk_bf16_f32 v60, v136, v137\n"
        "v_cvt_pk_bf16_f32 v61, v138, v139\n"
        "v_cvt_pk_bf16_f32 v62, v140, v141\n"
        "v_cvt_pk_bf16_f32 v63, v142, v143\n"
        "v_lshlrev_b32 v128, 16, v56\n"
        "v_and_b32 v129, 0xffff0000, v56\n"
        "v_lshlrev_b32 v130, 16, v57\n"
        "v_and_b32 v131, 0xffff0000, v57\n"
        "v_lshlrev_b32 v132, 16, v58\n"
        "v_and_b32 v133, 0xffff0000, v58\n"
        "v_lshlrev_b32 v134, 16, v59\n"
        "v_and_b32 v135, 0xffff0000, v59\n"
        "v_lshlrev_b32 v136, 16, v60\n"
        "v_and_b32 v137, 0xffff0000, v60\n"
        "v_lshlrev_b32 v138, 16, v61\n"
        "v_and_b32 v139, 0xffff0000, v61\n"
        "v_lshlrev_b32 v140, 16, v62\n"
        "v_and_b32 v141, 0xffff0000, v62\n"
        "v_lshlrev_b32 v142, 16, v63\n"
        "v_and_b32 v143, 0xffff0000, v63\n"
        "v_pk_add_f32 v[128:129], v[128:129], v[40:41]\n"
        "v_pk_add_f32 v[130:131], v[130:131], v[42:43]\n"
        "v_pk_add_f32 v[132:133], v[132:133], v[44:45]\n"
        "v_pk_add_f32 v[134:135], v[134:135], v[46:47]\n"
        "v_pk_add_f32 v[136:137], v[136:137], v[48:49]\n"
        "v_pk_add_f32 v[138:139], v[138:139], v[50:51]\n"
        "v_pk_add_f32 v[140:141], v[140:141], v[52:53]\n"
        "v_pk_add_f32 v[142:143], v[142:143], v[54:55]\n"
        "v_cvt_pk_bf16_f32 v56, v128, v129\n"
        "v_cvt_pk_bf16_f32 v57, v130, v131\n"
        "v_cvt_pk_bf16_f32 v58, v132, v133\n"
        "v_cvt_pk_bf16_f32 v59, v134, v135\n"
        "v_cvt_pk_bf16_f32 v60, v136, v137\n"
        "v_cvt_pk_bf16_f32 v61, v138, v139\n"
        "v_cvt_pk_bf16_f32 v62, v140, v141\n"
        "v_cvt_pk_bf16_f32 v63, v142, v143\n"
        "s_cmp_eq_u32 %[sw], 0\n"
        "s_cbranch_scc1 .Lo2%=\n"
        "s_andn2_b32 exec_lo, 0x0fffffff, s56\n"
        "s_andn2_b32 exec_hi, 0x0fffffff, s57\n"
        "global_store_dwordx4 v26, v[56:59], %[out] sc1\n"
        "global_store_dwordx4 v26, v[60:63], %[out] offset:448 sc1\n"
        "s_mov_b64 exec, -1\n"
        "s_mov_b32 s40, %[fn]\n"
        "s_mov_b32 s50, %[fstep]\n"
        "s_mov_b32 s51, 0\n"
        "s_mov_b32 m0, %[fl]\n"
        "s_cmp_eq_u32 s40, 0\n"
        "s_cbranch_scc1 .Lgd%=\n"
        ".Lg%=:\n"
        "global_load_lds_dwordx4 v[12:13], off\n"
        "s_add_u32 m0, m0, 0xc00\n"
        "v_lshl_add_u64 v[12:13], v[12:13], 0, s[50:51]\n"
        "s_sub_u32 s40, s40, 1\n"
        "s_cmp_lg_u32 s40, 0\n"
        "s_cbranch_scc1 .Lg%=\n"
        ".Lgd%=:\n"
        "s_cmp_lg_u32 %[role], 1\n"
        "s_cbranch_scc1 .Lno%=\n"
        "v_mov_b32 v96, 0x3f803f80\n"
        "v_mov_b32 v97, 0x3f803f80\n"
        "v_mov_b32 v98, 0x3f803f80\n"
        "v_mov_b32 v99, 0x3f803f80\n"
        "ds_write_b128 v35, v[96:99]\n"
        ".Lno%=:\n"
        "v_mov_b32 v118, %[lr]\n"
        "s_waitcnt vmcnt(0) lgkmcnt(0)\n"
        "s_barrier\n"
        "ds_read_b64 v[116:117], v118 offset:64\n"
        "s_waitcnt lgkmcnt(0)\n"
        "s_branch .Lo3%=\n"
        ".Lo2%=:\n"
        "s_mov_b64 s[56:57], -1\n"
        ".Lo3%=:\n"
        "v_and_b32 v114, 0x7ff, v116\n"
        "v_and_b32 v115, 0x7ff, v117\n"
        "v_cmp_ne_u32 vcc, v114, v28\n"
        "v_cmp_ne_u32 s[46:47], v115, v28\n"
        "s_and_b64 vcc, vcc, s[46:47]\n"
        "s_and_b64 vcc, vcc, s[56:57]\n"
        "s_and_b32 exec_lo, vcc_lo, 0x0fffffff\n"
        "s_and_b32 exec_hi, vcc_hi, 0x0fffffff\n"
        "global_store_dwordx4 v26, v[56:59], %[out] sc1\n"
        "global_store_dwordx4 v26, v[60:63], %[out] offset:448 sc1\n"
        "s_mov_b64 exec, -1\n"
        "s_cmp_lg_u32 %[role], 2\n"
        "s_cbranch_scc1 .Lend%=\n"
        "ds_read_b64_tr_b16 v[128:129], v33 offset:0\n"
        "ds_read_b64_tr_b16 v[130:131], v33 offset:128\n"
        "ds_read_b64_tr_b16 v[132:133], v33 offset:1024\n"
        "ds_read_b64_tr_b16 v[134:135], v33 offset:1152\n"
        "ds_read_b64_tr_b16 v[136:137], v33 offset:2048\n"
        "ds_read_b64_tr_b16 v[138:139], v33 offset:2176\n"
        "ds_read_b64_tr_b16 v[140:141], v33 offset:3072\n"
        "ds_read_b64_tr_b16 v[142:143], v33 offset:3200\n"
        "ds_read_b64_tr_b16 v[144:145], v33 offset:4096\n"
        "ds_read_b64_tr_b16 v[146:147], v33 offset:4224\n"
        "ds_read_b64_tr_b16 v[148:149], v33 offset:5120\n"
        "ds_read_b64_tr_b16 v[150:151], v33 offset:5248\n"
        "ds_read_b64_tr_b16 v[152:153], v33 offset:6144\n"
        "ds_read_b64_tr_b16 v[154:155], v33 offset:6272\n"
        "ds_read_b64_tr_b16 v[156:157], v33 offset:7168\n"
        "ds_read_b64_tr_b16 v[158:159], v33 offset:7296\n"
        "ds_read_b64_tr_b16 v[160:161], v33 offset:8192\n"
        "ds_read_b64_tr_b16 v[162:163], v33 offset:8320\n"
        "ds_read_b64_tr_b16 v[164:165], v33 offset:9216\n"
        "ds_read_b64_tr_b16 v[166:167], v33 offset:9344\n"
        "ds_read_b64_tr_b16 v[168:169], v33 offset:10240\n"
        "ds_read_b64_tr_b16 v[170:171], v33 offset:10368\n"
        "ds_read_b64_tr_b16 v[172:173], v33 offset:11264\n"
        "ds_read_b64_tr_b16 v[174:175], v33 offset:11392\n"
        "ds_read_b64_tr_b16 v[176:177], v33 offset:12288\n"
        "ds_read_b64_tr_b16 v[178:179], v33 offset:12416\n"
        "ds_read_b64_tr_b16 v[180:181], v33 offset:13312\n"
        "ds_read_b64_tr_b16 v[182:183], v33 offset:13440\n"
        "v_and_b32 v92, 2, v36\n"
        "v_lshlrev_b32 v92, 3, v92\n"
        "s_mul_i32 s46, %[sw], 1920\n"
        "s_sub_u32 s46, 0x2000, s46\n"
        "v_add_u32 v91, s46, v35\n"
        "s_cmp_lg_u32 %[sw], 0\n"
        "s_cbranch_scc1 .Lfo2%=\n"
        "s_mov_b32 s40, 0x10000\n"
        ".Lfr%=:\n"
        "s_waitcnt vmcnt(0)\n"
        "v_cmp_ne_u32 vcc, %[tag], v97\n"
        "v_cmp_ne_u32 s[46:47], %[tag], v123\n"
        "s_or_b64 vcc, vcc, s[46:47]\n"
        "s_cbranch_scc0 .Lfo%=\n"
        "buffer_load_dwordx2 v[122:123], v125, %[rs], s54 offen offset:16 sc1\n"
        "buffer_load_dwordx2 v[96:97], v124, %[rs], %[hrow] offen sc1\n"
        "s_sub_u32 s40, s40, 1\n"
        "s_cmp_lg_u32 s40, 0\n"
        "s_cbranch_scc1 .Lfr%=\n"
        ".Lfo%=:\n"
        "v_lshrrev_b32 v40, v92, v96\n"
        "v_lshlrev_b32 v40, 16, v40\n"
        "v_lshlrev_b32 v41, 16, v122\n"
        "v_and_b32 v42, 0xffff0000, v122\n"
        "v_rcp_f32 v42, v42\n"
        "v_sub_f32 v41, v40, v41\n"
        "v_cvt_pk_bf16_f32 v41, v41, v41\n"
        "v_lshlrev_b32 v41, 16, v41\n"
        "v_mul_f32 v41, v41, v42\n"
        "v_cvt_pk_bf16_f32 v41, v41, v41\n"
        "v_lshlrev_b32 v41, 16, v41\n"
        "v_mul_u32_u24 v44, 0x380, v119\n"
        "v_add_u32 v44, v44, v36\n"
        "v_and_b32 v43, 0xfffff800, v120\n"
        "v_ashrrev_i32 v45, 31, v43\n"
        "v_not_b32 v45, v45\n"
        "v_and_b32 v45, 0x7ffff800, v45\n"
        "v_or_b32 v45, 0x80000000, v45\n"
        "v_xor_b32 v43, v43, v45\n"
        "v_cvt_pk_bf16_f32 v43, v43, v43\n"
        "v_lshlrev_b32 v43, 16, v43\n"
        "v_mul_f32 v43, 0xbfb8aa3b, v43\n"
        "v_exp_f32 v43, v43\n"
        "s_nop 0\n"
        "v_add_f32 v43, 1.0, v43\n"
        "v_rcp_f32 v43, v43\n"
        "s_nop 0\n"
        "v_cvt_pk_bf16_f32 v43, v43, v43\n"
        "v_lshlrev_b32 v43, 16, v43\n"
        ".Lfo2%=:\n"
        "s_mov_b32 s40, 0x10000\n"
        "s_movk_i32 s41, 0x1000\n"
        "s_mov_b64 s[44:45], -1\n"
        "s_mov_b64 s[46:47], -1\n"
        "s_mov_b64 s[48:49], -1\n"
        "s_mov_b32 s50, -1\n"
        "s_mov_b32 s51, 0\n"
        "v_mov_b32 v109, %[tag]\n"
        "v_mov_b32 v111, %[tag]\n"
        "v_mov_b32 v46, 0x05040100\n"
        "v_mov_b32 v47, 0x07060302\n"
        ".La%=:\n"
        "s_mov_b64 exec, s[44:45]\n"
        "buffer_load_dwordx4 v[96:99], v34, %[rs], 0 offen offset:0 sc1\n"
        "s_mov_b64 exec, s[46:47]\n"
        "buffer_load_dwordx4 v[100:103], v34, %[rs], 0 offen offset:2048 sc1\n"
        "s_mov_b64 exec, s[48:49]\n"
        "buffer_load_dwordx4 v[104:107], v34, %[rs], s41 offen offset:0 sc1\n"
        "s_mov_b64 exec, s[50:51]\n"
        "buffer_load_dwordx4 v[108:111], v34, %[rs], s41 offen offset:2048 sc1\n"
        "s_mov_b64 exec, -1\n"
        "s_sub_u32 s40, s40, 1\n"
        "s_waitcnt vmcnt(3)\n"
        "v_cmp_ne_u32 s[44:45], %[tag], v97\n"
        "v_cmp_ne_u32 vcc, %[tag], v99\n"
        "s_or_b64 s[44:45], s[44:45], vcc\n"
        "s_waitcnt vmcnt(2)\n"
        "v_cmp_ne_u32 s[46:47], %[tag], v101\n"
        "v_cmp_ne_u32 vcc, %[tag], v103\n"
        "s_or_b64 s[46:47], s[46:47], vcc\n"
        "s_waitcnt vmcnt(1)\n"
        "v_cmp_ne_u32 s[48:49], %[tag], v105\n"
        "v_cmp_ne_u32 vcc, %[tag], v107\n"
        "s_or_b64 s[48:49], s[48:49], vcc\n"
        "s_waitcnt vmcnt(0)\n"
        "v_cmp_ne_u32 s[50:51], %[tag], v109\n"
        "v_cmp_ne_u32 vcc, %[tag], v111\n"
        "s_or_b64 s[50:51], s[50:51], vcc\n"
        "s_cmp_eq_u32 s40, 0\n"
        "s_cbranch_scc1 .Lad%=\n"
        "s_or_b64 s[52:53], s[44:45], s[46:47]\n"
        "s_or_b64 vcc, s[48:49], s[50:51]\n"
        "s_or_b64 s[52:53], s[52:53], vcc\n"
        "s_cbranch_scc1 .La%=\n"
        ".Lad%=:\n"
        PROF_STAMP_ASM(12)
        "v_perm_b32 v112, v98, v96, v46\n"
        "v_perm_b32 v113, v98, v96, v47\n"
        "v_perm_b32 v114, v102, v100, v46\n"
        "v_perm_b32 v115, v102, v100, v47\n"
        "v_perm_b32 v116, v106, v104, v46\n"
        "v_perm_b32 v117, v106, v104, v47\n"
        "v_perm_b32 v118, v110, v108, v46\n"
        "v_perm_b32 v119, v110, v108, v47\n"
        "ds_write2st64_b32 v35, v112, v113 offset0:0 offset1:4\n"
        "ds_write2st64_b32 v35, v114, v115 offset0:1 offset1:5\n"
        "ds_write2st64_b32 v35, v116, v117 offset0:2 offset1:6\n"
        "s_mov_b32 exec_hi, 0\n"
        "ds_write2st64_b32 v35, v118, v119 offset0:3 offset1:7\n"
        "s_mov_b32 exec_hi, -1\n"
        "ds_read_b128 v[184:187], v32 offset:0\n"
        "ds_read_b128 v[188:191], v32 offset:64\n"
        "ds_read_b128 v[192:195], v32 offset:128\n"
        "ds_read_b128 v[196:199], v32 offset:192\n"
        "ds_read_b128 v[200:203], v32 offset:256\n"
        "ds_read_b128 v[204:207], v32 offset:320\n"
        "ds_read_b128 v[208:211], v32 offset:384\n"
        "ds_read_b128 v[212:215], v32 offset:448\n"
        "ds_read_b128 v[216:219], v32 offset:512\n"
        "ds_read_b128 v[220:223], v32 offset:576\n"
        "ds_read_b128 v[224:227], v32 offset:640\n"
        "ds_read_b128 v[228:231], v32 offset:704\n"
        "ds_read_b128 v[232:235], v32 offset:768\n"
        "ds_read_b128 v[236:239], v32 offset:832\n"
        "s_waitcnt lgkmcnt(12)\n"
        "v_mfma_f32_16x16x32_bf16 v[240:243], v[184:187], v[128:131], 0\n"
        "v_mfma_f32_16x16x32_bf16 v[244:247], v[188:191], v[132:135], 0\n"
        "s_waitcnt lgkmcnt(10)\n"
        "v_mfma_f32_16x16x32_bf16 v[240:243], v[192:195], v[136:139], v[240:243]\n"
        "v_mfma_f32_16x16x32_bf16 v[244:247], v[196:199], v[140:143], v[244:247]\n"
        "s_waitcnt lgkmcnt(8)\n"
        "v_mfma_f32_16x16x32_bf16 v[240:243], v[200:203], v[144:147], v[240:243]\n"
        "v_mfma_f32_16x16x32_bf16 v[244:247], v[204:207], v[148:151], v[244:247]\n"
        "s_waitcnt lgkmcnt(6)\n"
        "v_mfma_f32_16x16x32_bf16 v[240:243], v[208:211], v[152:155], v[240:243]\n"
        "v_mfma_f32_16x16x32_bf16 v[244:247], v[212:215], v[156:159], v[244:247]\n"
        "s_waitcnt lgkmcnt(4)\n"
        "v_mfma_f32_16x16x32_bf16 v[240:243], v[216:219], v[160:163], v[240:243]\n"
        "v_mfma_f32_16x16x32_bf16 v[244:247], v[220:223], v[164:167], v[244:247]\n"
        "s_waitcnt lgkmcnt(2)\n"
        "v_mfma_f32_16x16x32_bf16 v[240:243], v[224:227], v[168:171], v[240:243]\n"
        "v_mfma_f32_16x16x32_bf16 v[244:247], v[228:231], v[172:175], v[244:247]\n"
        "s_waitcnt lgkmcnt(0)\n"
        "v_mfma_f32_16x16x32_bf16 v[240:243], v[232:235], v[176:179], v[240:243]\n"
        "v_mfma_f32_16x16x32_bf16 v[244:247], v[236:239], v[180:183], v[244:247]\n"
        "s_nop 7\n"
        "v_pk_add_f32 v[240:241], v[240:241], v[244:245]\n"
        "s_mov_b64 exec, 0xffff\n"
        "ds_write2_b32 v91, v240, v241 offset1:16\n"
        "s_mov_b64 exec, -1\n"
        "s_waitcnt lgkmcnt(0)\n"
        "s_barrier\n"
        "s_cmp_lg_u32 %[sw], 0\n"
        "s_cbranch_scc1 .Lend%=\n"
        "ds_read2_b32 v[48:49], v91 offset1:32\n"
        "ds_read2_b32 v[50:51], v91 offset0:64 offset1:96\n"
        "s_waitcnt lgkmcnt(0)\n"
        "v_pk_add_f32 v[48:49], v[48:49], v[50:51]\n"
        "v_add_f32 v48, v48, v49\n"
        "v_cvt_pk_bf16_f32 v48, v48, v48\n"
        "v_lshlrev_b32 v48, 16, v48\n"
        "v_mul_f32 v48, v48, v43\n"
        "v_cvt_pk_bf16_f32 v48, v48, v48\n"
        "v_lshlrev_b32 v48, 16, v48\n"
        "v_add_f32 v48, v48, v41\n"
        "v_cvt_pk_bf16_f32 v48, v48, v48\n"
        "v_lshlrev_b32 v48, 16, v48\n"
        "v_add_f32 v48, v48, v40\n"
        "v_cvt_pk_bf16_f32 v48, v48, v48\n"
        "s_mov_b32 exec_hi, 0\n"
        "global_store_short v44, v48, %[out] sc1\n"
        "s_mov_b32 exec_hi, -1\n"
        PROF_STAMP_ASM(13)
        ".Lend%=:\n"
        : "+{v[4:15]}"(p2), "+{v[80:87]}"(cv) PROF_ASM_OUT3
        : "{v[64:79]}"(ht), "{v[24:39]}"(tp), [rs] "s"(rs), [tag] "s"(tag), [sq] "s"(WS_QF + wg * 8192), [sw] "s"(w), [role] "s"(role),
          [lr] "s"(lb + K2_R), [hrow] "s"(WS_HROW), [out] "s"(out), [c1792] "s"(1792),
          [fl] "s"(lb + K2_FW + (w - 1) * 1024), [fstep] "s"(96 * 896), [fn] "s"(__builtin_amdgcn_readfirstlane(st2 ? (w == 3 ? 18 : 19) : 0))
        : "v40", "v41", "v42", "v43", "v44", "v45", "v46", "v47", "v48", "v49", "v50", "v51", "v52", "v53", "v54", "v55", "v56", "v57", "v58", "v59", "v60", "v61", "v62", "v63", "v88", "v89", "v90", "v91", "v92", "v93", "v94", "v95", "v96", "v97", "v98", "v99", "v100", "v101", "v102", "v103", "v104", "v105", "v106", "v107", "v108", "v109", "v110", "v111", "v112", "v113", "v114", "v115", "v116", "v117", "v118", "v119", "v120", "v121", "v122", "v123", "v124", "v125", "v126", "v127", "v128", "v129", "v130", "v131", "v132", "v133", "v134", "v135", "v136", "v137", "v138", "v139", "v140", "v141", "v142", "v143", "v144", "v145", "v146", "v147", "v148", "v149", "v150", "v151", "v152", "v153", "v154", "v155", "v156", "v157", "v158", "v159", "v160", "v161", "v162", "v163", "v164", "v165", "v166", "v167", "v168", "v169", "v170", "v171", "v172", "v173", "v174", "v175", "v176", "v177", "v178", "v179", "v180", "v181", "v182", "v183", "v184", "v185", "v186", "v187", "v188", "v189", "v190", "v191", "v192", "v193", "v194", "v195", "v196", "v197", "v198", "v199", "v200", "v201", "v202", "v203", "v204", "v205", "v206", "v207", "v208", "v209", "v210", "v211", "v212", "v213", "v214", "v215", "v216", "v217", "v218", "v219", "v220", "v221", "v222", "v223", "v224", "v225", "v226", "v227", "v228", "v229", "v230", "v231", "v232", "v233", "v234", "v235", "v236", "v237", "v238", "v239", "v240", "v241", "v242", "v243", "v244", "v245", "v246", "v247", "v248", "v249", "v250", "v251", "v252", "v253", "v254", "v255", "s40", "s41", "s42", "s43", "s44", "s45", "s46", "s47", "s48", "s49", "s50", "s51", "s52", "s53", "s54", "s55", "s56", "s57", "s58", "s59", "scc", "vcc", "memory");
    STAMP(14);
    PROF_FLUSH(0);
}

extern "C" void kernel_launch(void* const* d_in, const int* in_sizes, int n_in, void* d_out, int out_size, void* d_ws, size_t ws_size,
                              hipStream_t stream) {
    const u16* x = (const u16*)d_in[0];
    const u16* wq = (const u16*)d_in[1];
    const u16* wk = (const u16*)d_in[2];
    const u16* wv = (const u16*)d_in[3];
    const u16* wo = (const u16*)d_in[4];
    const u16* wr = (const u16*)d_in[6];
    const u16* wgate = (const u16*)d_in[7];
    const u16* wup = (const u16*)d_in[8];
    const u16* wdown = (const u16*)d_in[9];
    char* ws = (char*)d_ws;
    u16* out = (u16*)d_out;
    (void)hipFuncSetAttribute((const void*)fused_block, hipFuncAttributeMaxDynamicSharedMemorySize, LDS_BYTES);
    fused_block<<<256, 256, LDS_BYTES, stream>>>(x, wq, wk, wv, wo, wr, wgate, wup, wdown, ws, out);
}
```
